```python
import torch

def _softmax(x, axis=-1):
    return torch.softmax(x, dim=axis)
def _gelu(x):
    return torch.nn.functional.gelu(x, approximate="tanh")
def _silu(x):
    return torch.nn.functional.silu(x)
def _sigmoid(x):
    return torch.sigmoid(x)
def _layernorm(x, axis=-1, eps=1e-5):
    m = x.mean(dim=axis, keepdim=True)
    v = x.var(dim=axis, keepdim=True, unbiased=False)
    return (x - m) / torch.sqrt(v + eps)
def _scatter_add(tgt, idx, src, axis=0):
    return tgt.clone().index_add_(axis, idx, src)
def _segment_sum(src, seg, n):
    out = torch.zeros((n,) + src.shape[1:], dtype=src.dtype)
    return out.index_add_(0, seg, src)
def _pad(x, pw):
    flat = tuple(v for pair in reversed(pw) for v in pair)
    return torch.nn.functional.pad(x, flat)

kernel_name = 'standalone__geglu_ffn__prompt'

def setup_inputs():

    _w = (lambda a: (lambda t: t.to(torch.bfloat16) if t.is_floating_point() else t)(torch.as_tensor(a)))
    B, S, D, F = 1, 2048, 512, 1792
    return {
        "x": _w((lambda *s: torch.randn(*s, dtype=torch.bfloat16) * 0.05)(B, S, D)),
        "w_gate": _w((lambda *s: torch.randn(*s, dtype=torch.bfloat16) * 0.05)(D, F)),
        "w_up": _w((lambda *s: torch.randn(*s, dtype=torch.bfloat16) * 0.05)(D, F)),
        "w_down": _w((lambda *s: torch.randn(*s, dtype=torch.bfloat16) * 0.05)(F, D)),
    }

def reference(x, w_gate, w_up, w_down):
    return (_gelu(x @ w_gate) * (x @ w_up)) @ w_down
```

```cpp
#include <hip/hip_runtime.h>
#include <hip/hip_bf16.h>
typedef short bf16x8 __attribute__((ext_vector_type(8)));
typedef float f32x16 __attribute__((ext_vector_type(16)));
typedef unsigned short u16;
constexpr int M=2048, D=512, F=1792;

__device__ __forceinline__ u16 f2bf(float x){ __hip_bfloat16 b = __float2bfloat16(x); return *reinterpret_cast<u16*>(&b); }
__device__ __forceinline__ float gelu_mul(float g, float u){
  const float A = -2.0f*0.7978845608028654f*1.4426950408889634f;
  const float B = A*0.044715f;
  float t = g*(A + B*g*g);
  float e = __builtin_amdgcn_exp2f(t);
  return g*u*__builtin_amdgcn_rcpf(1.0f+e);
}
__global__ __launch_bounds__(64) void k1(const u16* x, const u16* wg, const u16* wu, u16* h){
  int lane = threadIdx.x, r = lane&31, hh = lane>>5;
  int tg = blockIdx.x / (F/16), fg = blockIdx.x % (F/16);
  const u16* w = (r<16)? wg : wu;
  int f = fg*16 + (r&15);
  f32x16 acc = {};
  for(int s=0;s<D/16;s++){
    bf16x8 a,b;
    for(int j=0;j<8;j++) a[j] = w[(size_t)(16*s+8*hh+j)*F + f];
    b = *reinterpret_cast<const bf16x8*>(x + (size_t)(tg*32+r)*D + 16*s+8*hh);
    acc = __builtin_amdgcn_mfma_f32_32x32x16_bf16(a,b,acc,0,0,0);
  }
  for(int i=0;i<8;i++){
    int fr = (i&3) + 8*(i>>2) + 4*hh;
    h[(size_t)(tg*32+r)*F + fg*16 + fr] = f2bf(gelu_mul(acc[i], acc[i+8]));
  }
}
__global__ __launch_bounds__(64) void k2(const u16* h, const u16* wd, u16* out){
  int lane = threadIdx.x, r = lane&31, hh = lane>>5;
  int tg = blockIdx.x / (D/32), dg = blockIdx.x % (D/32);
  f32x16 acc = {};
  for(int s=0;s<F/16;s++){
    bf16x8 a,b;
    for(int j=0;j<8;j++) a[j] = wd[(size_t)(16*s+8*hh+j)*D + dg*32 + r];
    b = *reinterpret_cast<const bf16x8*>(h + (size_t)(tg*32+r)*F + 16*s+8*hh);
    acc = __builtin_amdgcn_mfma_f32_32x32x16_bf16(a,b,acc,0,0,0);
  }
  for(int i=0;i<16;i++){
    int dr = (i&3) + 8*(i>>2) + 4*hh;
    out[(size_t)(tg*32+r)*D + dg*32 + dr] = f2bf(acc[i]);
  }
}
extern "C" void kernel_launch(void* const* d_in, const int* in_sizes, int n_in,
                              void* d_out, int out_size, void* d_ws, size_t ws_size,
                              hipStream_t stream) {
  const u16* x=(const u16*)d_in[0]; const u16* wg=(const u16*)d_in[1]; const u16* wu=(const u16*)d_in[2]; const u16* wd=(const u16*)d_in[3];
  u16* h=(u16*)d_ws;
  k1<<<(M/32)*(F/16),64,0,stream>>>(x,wg,wu,h);
  k2<<<(M/32)*(D/32),64,0,stream>>>(h,wd,(u16*)d_out);
}
```

```cpp
#include <hip/hip_runtime.h>

__global__ __launch_bounds__(256, 1) __attribute__((aligned(4096))) void geglu_ffn_fused(const void* x, const void* wg, const void* wu, const void* wd,
                                                          void* out, void* ws) {
  __shared__ __attribute__((aligned(16))) char lds[163840];
  asm volatile(
    "v_lshrrev_b32 v2, 6, %2\n"
    "s_load_dwordx2 s[4:5], %0, 0\n"
    "s_load_dwordx2 s[20:21], %0, 40\n"
    "v_readfirstlane_b32 s36, v2\n"
    "s_lshr_b32 s38, %1, 3\n"
    "s_and_b32 s37, %1, 3\n"
    "s_and_b32 s96, %1, 4\n"
    "s_and_b32 s33, s38, 3\n"
    "s_or_b32 s33, s33, s96\n"
    "v_and_b32 v1, 63, %2\n"
    "s_mov_b32 s7, 0x20000\n"
    "s_mov_b32 s11, 0x20000\n"
    "s_lshl_b32 s53, s36, 10\n"
    "s_cmp_lt_u32 s38, 4\n"
    "s_cbranch_scc1 L_idle\n"
    "s_lshl_b32 s97, s36, 2\n"
    "s_and_b32 s97, s97, 8\n"
    "s_add_u32 s97, s97, 8\n"
    "s_load_dwordx2 s[8:9], %0, s97\n"
    "s_lshr_b32 s34, s38, 2\n"
    "s_mul_i32 s99, s37, 7\n"
    "s_add_u32 s34, s34, s99\n"
    "s_sub_u32 s34, s34, 1\n"
    "s_mov_b32 s6, 2097152\n"
    "s_mov_b32 s10, 1835008\n"
    "v_lshrrev_b32 v3, 3, %2\n"
    "v_lshrrev_b32 v5, 4, %2\n"
    "v_xor_b32 v5, v5, %2\n"
    "v_and_b32 v5, 7, v5\n"
    "v_lshlrev_b32 v5, 4, v5\n"
    "v_lshl_add_u32 v7, v3, 10, v5\n"
    "s_lshl_b32 s96, s33, 2\n"
    "s_add_u32 s44, s96, s37\n"
    "v_lshl_add_u32 v178, s44, 16, v7\n"
    "s_add_u32 s98, s33, s34\n"
    "s_and_b32 s98, s98, 1\n"
    "v_lshl_add_u32 v178, s98, 15, v178\n"
    "v_xor_b32 v179, 0x8000, v178\n"
    "v_xor_b32 v180, 0x10000, v178\n"
    "v_xor_b32 v181, 0x18000, v178\n"
    "v_and_b32 v4, 2, v3\n"
    "v_and_b32 v3, 15, v3\n"
    "v_mul_u32_u24 v3, 3584, v3\n"
    "v_lshrrev_b32 v5, 4, %2\n"
    "v_and_b32 v5, 4, v5\n"
    "v_and_b32 v6, 7, %2\n"
    "v_xor_b32 v4, v4, v5\n"
    "v_xor_b32 v4, v4, v6\n"
    "v_lshl_add_u32 v3, v4, 4, v3\n"
    "v_lshl_add_u32 v150, s34, 7, v3\n"
    "s_add_u32 s98, s33, s34\n"
    "s_and_b32 s98, s98, 1\n"
    "s_lshr_b32 s97, %1, 2\n"
    "s_xor_b32 s97, %1, s97\n"
    "s_and_b32 s97, s97, 1\n"
    "s_lshl_b32 s97, s97, 1\n"
    "s_or_b32 s98, s98, s97\n"
    "s_lshl_b32 s98, s98, 7\n"
    "s_xor_b32 s24, s98, 512\n"
    "s_xor_b32 s25, s98, 640\n"
    "s_add_u32 m0, s53, 16384\n"
    "s_waitcnt lgkmcnt(0)\n"
    "buffer_load_dwordx4 v178, s[4:7], s24 offen lds\n"
    "s_load_dwordx2 s[12:13], %0, 24\n"
    "s_load_dwordx2 s[16:17], %0, 32\n"
    "s_load_dwordx2 s[56:57], s[20:21], 0\n"
    "s_add_u32 m0, s53, 20480\n"
    "s_mov_b32 s14, 1835008\n"
    "buffer_load_dwordx4 v179, s[4:7], s24 offen lds\n"
    "s_add_u32 m0, s53, 24576\n"
    "s_mov_b32 s15, 0x20000\n"
    "buffer_load_dwordx4 v180, s[4:7], s24 offen lds\n"
    "s_add_u32 m0, s53, 28672\n"
    "s_mov_b32 s18, 2097152\n"
    "buffer_load_dwordx4 v181, s[4:7], s24 offen lds\n"
    "s_mul_i32 s52, s24, 1792\n"
    "s_add_u32 m0, s53, 0\n"
    "s_mov_b32 s19, 0x20000\n"
    "buffer_load_dwordx4 v150, s[8:11], s52 offen lds\n"
    "s_add_u32 s52, s52, 57344\n"
    "s_add_u32 m0, s53, 4096\n"
    "s_mov_b32 s22, 13631488\n"
    "buffer_load_dwordx4 v150, s[8:11], s52 offen lds\n"
    "s_add_u32 s52, s52, 57344\n"
    "s_add_u32 m0, s53, 8192\n"
    "s_mov_b32 s23, 0x20000\n"
    "buffer_load_dwordx4 v150, s[8:11], s52 offen lds\n"
    "s_add_u32 s52, s52, 57344\n"
    "s_add_u32 m0, s53, 12288\n"
    "s_nop 0\n"
    "buffer_load_dwordx4 v150, s[8:11], s52 offen lds\n"
    "s_branch L_work2\n"
    "L_idle:\n"
    "s_memtime s[82:83]\n"
    "s_lshl_b32 s98, s36, 2\n"
    "s_add_u32 s98, s98, s38\n"
    "s_cmp_lg_u32 s98, 15\n"
    "s_cbranch_scc1 L_pf\n"
    "s_getpc_b64 s[92:93]\n"
    "L_pc:\n"
    "v_lshlrev_b32 v2, 7, v1\n"
    "v_min_u32 v3, L_end-L_pc-4, v2\n"
    "global_load_dword v20, v3, s[92:93]\n"
    "v_add_u32 v2, 0x2000, v2\n"
    "v_min_u32 v3, L_end-L_pc-4, v2\n"
    "global_load_dword v21, v3, s[92:93]\n"
    "v_add_u32 v2, 0x2000, v2\n"
    "v_min_u32 v3, L_end-L_pc-4, v2\n"
    "global_load_dword v22, v3, s[92:93]\n"
    "v_add_u32 v2, 0x2000, v2\n"
    "L_pf:\n"
    "s_cmp_ge_u32 s98, 7\n"
    "s_cselect_b32 s97, 16, 8\n"
    "s_load_dwordx2 s[8:9], %0, s97\n"
    "s_cselect_b32 s92, 7, 0\n"
    "s_sub_u32 s99, s98, s92\n"
    "s_mul_i32 s92, s37, 7\n"
    "s_add_u32 s99, s99, s92\n"
    "s_lshl_b32 s99, s99, 7\n"
    "s_cmp_lt_u32 s98, 14\n"
    "s_cselect_b32 s10, 1835008, 0\n"
    "s_mov_b32 s6, 2097152\n"
    "s_lshr_b32 s92, %1, 2\n"
    "s_xor_b32 s92, %1, s92\n"
    "s_and_b32 s92, s92, 1\n"
    "s_lshl_b32 s92, s92, 8\n"
    "s_xor_b32 s46, s92, 0\n"
    "v_mul_u32_u24 v9, 3584, v1\n"
    "v_add_u32 v9, s99, v9\n"
    "s_and_b32 s92, s36, 1\n"
    "s_xor_b32 s92, s92, s37\n"
    "s_lshl_b32 s93, s33, 2\n"
    "s_add_u32 s92, s92, s93\n"
    "s_lshl_b32 s92, s92, 16\n"
    "s_lshr_b32 s93, s36, 1\n"
    "s_lshl_b32 s93, s93, 7\n"
    "s_add_u32 s92, s92, s93\n"
    "v_lshl_add_u32 v8, v1, 10, s92\n"
    "s_mul_i32 s99, s46, 1792\n"
    "s_add_u32 s92, s99, 229376\n"
    "s_waitcnt lgkmcnt(0)\n"
    "s_sleep 10\n"
    "buffer_load_dword v10, v9, s[8:11], s99 offen\n"
    "buffer_load_dword v11, v8, s[4:7], s46 offen\n"
    "buffer_load_dword v12, v9, s[8:11], s92 offen\n"
    "s_branch L_idle2\n"
    "L_work2:\n"
    "s_xor_b32 s26, s98, 768\n"
    "s_xor_b32 s27, s98, 896\n"
    "s_xor_b32 s28, s98, 0\n"
    "s_xor_b32 s29, s98, 128\n"
    "s_xor_b32 s30, s98, 256\n"
    "s_xor_b32 s31, s98, 384\n"
    "v_and_b32 v3, 15, %2\n"
    "v_lshlrev_b32 v4, 7, v3\n"
    "v_lshrrev_b32 v5, 7, %2\n"
    "v_lshl_add_u32 v4, v5, 11, v4\n"
    "v_bfe_u32 v5, %2, 1, 3\n"
    "v_bfe_u32 v6, %2, 4, 2\n"
    "s_add_u32 m0, s53, 49152\n"
    "s_nop 0\n"
    "buffer_load_dwordx4 v178, s[4:7], s25 offen lds\n"
    "v_xor_b32 v5, v5, v6\n"
    "v_lshl_add_u32 v138, v5, 4, v4\n"
    "v_xor_b32 v141, 64, v138\n"
    "v_bfe_u32 v3, %2, 4, 1\n"
    "v_bfe_u32 v4, %2, 5, 1\n"
    "v_bfe_u32 v5, %2, 2, 2\n"
    "v_lshlrev_b32 v5, 7, v5\n"
    "v_lshl_add_u32 v5, v3, 10, v5\n"
    "v_lshl_add_u32 v4, v4, 12, v5\n"
    "v_and_b32 v6, 3, %2\n"
    "v_lshl_add_u32 v4, v6, 3, v4\n"
    "v_bfe_u32 v5, %2, 3, 2\n"
    "s_add_u32 m0, s53, 53248\n"
    "s_nop 0\n"
    "buffer_load_dwordx4 v179, s[4:7], s25 offen lds\n"
    "v_lshrrev_b32 v6, 5, %2\n"
    "v_and_b32 v6, 2, v6\n"
    "v_xor_b32 v5, v5, v6\n"
    "v_lshl_add_u32 v144, v5, 5, v4\n"
    "v_xor_b32 v147, 32, v144\n"
    "v_add_u32 v139, 65536, v138\n"
    "v_add_u32 v142, 65536, v141\n"
    "v_add_u32 v145, 65536, v144\n"
    "v_add_u32 v148, 65536, v147\n"
    "v_add_u32 v140, 131072, v138\n"
    "v_add_u32 v143, 131072, v141\n"
    "v_add_u32 v146, 131072, v144\n"
    "s_add_u32 m0, s53, 57344\n"
    "s_nop 0\n"
    "buffer_load_dwordx4 v180, s[4:7], s25 offen lds\n"
    "v_add_u32 v149, 131072, v147\n"
    "s_add_u32 s99, s33, s34\n"
    "s_and_b32 s99, s99, 1\n"
    "s_lshl_b32 s99, s99, 7\n"
    "s_mov_b32 s96, 0x20000\n"
    "v_xor_b32 v182, s96, v178\n"
    "v_xor_b32 v183, s96, v179\n"
    "v_xor_b32 v184, s96, v180\n"
    "v_xor_b32 v185, s96, v181\n"
    "s_lshl_b32 s96, s33, 5\n"
    "s_add_u32 s96, s96, s34\n"
    "s_lshl_b32 s96, s96, 2\n"
    "s_add_u32 m0, s53, 61440\n"
    "s_nop 0\n"
    "buffer_load_dwordx4 v181, s[4:7], s25 offen lds\n"
    "s_add_u32 s96, s96, s36\n"
    "s_lshl_b32 s96, s96, 9\n"
    "s_add_u32 s91, s96, 65536\n"
    "s_lshl_b32 s97, s34, 13\n"
    "s_add_u32 s97, s97, 1048576\n"
    "s_lshl_b32 s48, s37, 7\n"
    "s_add_u32 s48, s48, s91\n"
    "s_xor_b32 s51, s48, 384\n"
    "s_xor_b32 s47, s44, 3\n"
    "s_mul_i32 s47, s47, 229376\n"
    "s_add_u32 s47, s47, s97\n"
    "s_xor_b32 s50, s48, 256\n"
    "s_xor_b32 s46, s44, 2\n"
    "s_mul_i32 s46, s46, 229376\n"
    "s_add_u32 s46, s46, s97\n"
    "s_xor_b32 s49, s48, 128\n"
    "s_xor_b32 s45, s44, 1\n"
    "s_mul_i32 s45, s45, 229376\n"
    "s_add_u32 s45, s45, s97\n"
    "s_mul_i32 s44, s44, 229376\n"
    "s_add_u32 s44, s44, s97\n"
    "v_lshlrev_b32 v3, 4, v1\n"
    "v_bfe_u32 v4, %2, 7, 1\n"
    "v_lshl_add_u32 v3, v4, 10, v3\n"
    "v_bfe_u32 v4, %2, 6, 1\n"
    "v_lshl_add_u32 v155, v4, 12, v3\n"
    "v_lshl_add_u32 v186, s99, 4, v155\n"
    "v_xor_b32 v187, 0x800, v186\n"
    "v_mov_b32 v128, 0xc0135761\n"
    "v_mov_b32 v129, 0xc0135761\n"
    "v_mov_b32 v130, 0xbdd2d3e8\n"
    "v_mov_b32 v131, 0xbdd2d3e8\n"
    "s_lshr_b32 s99, s38, 2\n"
    "s_xor_b32 s99, s99, 3\n"
    "s_xor_b32 s65, s37, 2\n"
    "s_xor_b32 s92, s65, 1\n"
    "s_lshl_b32 s64, s36, 12\n"
    "s_mul_i32 s96, s65, 14\n"
    "s_add_u32 s96, s96, s36\n"
    "s_lshl_b32 s84, s96, 15\n"
    "s_add_u32 s85, s84, 131072\n"
    "s_add_u32 s86, s84, 262144\n"
    "s_add_u32 s87, s84, 393216\n"
    "s_mul_i32 s96, s92, 14\n"
    "s_add_u32 s96, s96, s36\n"
    "s_lshl_b32 s96, s96, 15\n"
    "s_add_u32 s88, s96, 65536\n"
    "s_add_u32 s89, s96, 196608\n"
    "s_add_u32 s90, s96, 327680\n"
    "s_sub_u32 s96, s96, 65536\n"
    "s_cmp_lt_u32 s36, 2\n"
    "s_cselect_b32 s87, s87, s96\n"
    "s_mov_b32 s58, 8192\n"
    "s_mov_b32 s59, 16384\n"
    "s_mov_b32 s60, 24576\n"
    "s_mov_b32 s61, 32768\n"
    "s_mov_b32 s62, 40960\n"
    "s_mov_b32 s63, 49152\n"
    "v_lshrrev_b32 v3, 3, v1\n"
    "v_and_b32 v5, 6, v3\n"
    "v_lshlrev_b32 v3, 10, v3\n"
    "v_lshl_add_u32 v3, s99, 7, v3\n"
    "v_and_b32 v4, 7, v1\n"
    "v_xor_b32 v4, v4, v5\n"
    "v_lshl_add_u32 v151, v4, 4, v3\n"
    "v_add_u32 v152, 7168, v151\n"
    "v_add_u32 v153, 14336, v151\n"
    "v_add_u32 v154, 21504, v151\n"
    "v_lshrrev_b32 v5, 2, v1\n"
    "v_lshlrev_b32 v5, 7, v5\n"
    "v_bfe_u32 v3, %2, 6, 1\n"
    "v_lshl_add_u32 v5, v3, 12, v5\n"
    "v_and_b32 v6, 3, %2\n"
    "v_lshl_add_u32 v5, v6, 3, v5\n"
    "v_bfe_u32 v4, %2, 3, 2\n"
    "v_lshl_add_u32 v163, v4, 5, v5\n"
    "v_xor_b32 v166, 32, v163\n"
    "v_xor_b32 v169, 64, v163\n"
    "v_xor_b32 v172, 96, v163\n"
    "v_add_u32 v164, 0x10000, v163\n"
    "v_add_u32 v165, 0x20000, v163\n"
    "v_add_u32 v167, 0x10000, v166\n"
    "v_add_u32 v168, 0x20000, v166\n"
    "v_add_u32 v170, 0x10000, v169\n"
    "v_add_u32 v171, 0x20000, v169\n"
    "v_add_u32 v173, 0x10000, v172\n"
    "v_add_u32 v174, 0x20000, v172\n"
    "s_lshl_b32 s93, s33, 2\n"
    "s_add_u32 s93, s93, s65\n"
    "s_lshl_b32 s96, s33, 16\n"
    "s_lshl_b32 s97, s65, 7\n"
    "s_add_u32 s96, s96, s97\n"
    "s_and_b32 s97, s36, 1\n"
    "s_lshl_b32 s97, s97, 9\n"
    "s_add_u32 s96, s96, s97\n"
    "s_add_u32 s96, s96, 65536\n"
    "v_min_u32 v2, 13, v1\n"
    "v_lshl_add_u32 v2, v2, 10, s96\n"
    "s_mul_i32 s96, s65, 14336\n"
    "s_mul_i32 s97, s92, 14336\n"
    "v_add_u32 v157, s96, v2\n"
    "v_add_u32 v158, s97, v2\n"
    "v_xor_b32 v159, 0x100, v157\n"
    "v_xor_b32 v160, 0x100, v158\n"
    "s_sub_u32 s42, s92, s65\n"
    "s_mul_i32 s42, s42, 57344\n"
    "s_xor_b32 s92, s93, 2\n"
    "s_sub_u32 s54, s92, s93\n"
    "s_mul_i32 s54, s54, 229376\n"
    "s_sub_u32 s54, s54, s42\n"
    "s_mov_b32 s98, s36\n"
    "s_lshl_b32 s96, s93, 2\n"
    "s_add_u32 s96, s96, s65\n"
    "s_mul_i32 s96, s96, 14\n"
    "s_and_b32 s97, s36, 1\n"
    "s_add_u32 s96, s96, s97\n"
    "s_lshl_b32 s96, s96, 2\n"
    "s_add_u32 s96, s96, s98\n"
    "s_lshl_b32 s96, s96, 10\n"
    "s_add_u32 s96, s96, 1048576\n"
    "v_lshlrev_b32 v136, 4, v1\n"
    "v_add_u32 v162, s96, v136\n"
    "v_xor_b32 v161, 0x400, v162\n"
    "s_mov_b32 s79, s98\n"
    "v_lshrrev_b32 v3, 5, %2\n"
    "v_min_u32 v3, 1, v3\n"
    "v_lshlrev_b32 v3, 31, v3\n"
    "v_lshl_or_b32 v156, %2, 2, v3\n"
    "s_waitcnt lgkmcnt(0)\n"
    "s_add_u32 s56, s56, 1\n"
    "s_addc_u32 s57, s57, 0\n"
    "s_and_b32 s39, s56, 1\n"
    "s_add_u32 s39, s39, 2\n"
    "s_lshl_b32 s39, s39, 24\n"
    "s_add_u32 s20, s20, s39\n"
    "s_addc_u32 s21, s21, 0\n"
    "v_mov_b32 v132, s56\n"
    "v_mov_b32 v3, s57\n"
    "v_and_b32 v4, 1, %2\n"
    "v_cmp_eq_u32 vcc, 1, v4\n"
    "s_lshl_b32 s35, %1, 7\n"
    "s_add_u32 s35, s35, 32768\n"
    "v_cndmask_b32 v133, v132, v3, vcc\n"
    "v_not_b32 v4, v132\n"
    "v_not_b32 v5, v132\n"
    "v_lshlrev_b32 v6, 3, v1\n"
    "v_lshrrev_b32 v3, 5, v1\n"
    "v_lshlrev_b32 v3, 31, v3\n"
    "v_lshl_or_b32 v137, v1, 2, v3\n"
    "s_waitcnt lgkmcnt(0)\n"
    "s_mov_b64 exec, 0\n"
    "s_cmp_eq_u32 s36, 0\n"
    "s_cbranch_scc1 L_stub0\n"
    "s_cmp_eq_u32 s36, 1\n"
    "s_cbranch_scc1 L_stub1\n"
    "s_cmp_eq_u32 s36, 2\n"
    "s_cbranch_scc1 L_stub2\n"
    "s_branch L_stub3\n"
    "L_warm_back:\n"
    "s_mov_b64 exec, -1\n"
    "s_waitcnt vmcnt(6)\n"
    "s_barrier\n"
    "ds_read_b64_tr_b16 a[0:1], v144 offset:0\n"
    "ds_read_b64_tr_b16 a[2:3], v144 offset:512\n"
    "ds_read_b128 v[80:83], v138 offset:16384\n"
    "ds_read_b128 v[84:87], v138 offset:20480\n"
    "ds_read_b128 v[88:91], v138 offset:24576\n"
    "ds_read_b128 v[92:95], v138 offset:28672\n"
    "ds_read_b64_tr_b16 a[4:5], v147 offset:0\n"
    "ds_read_b64_tr_b16 a[6:7], v147 offset:512\n"
    "s_mul_i32 s52, s25, 1792\n"
    "s_add_u32 m0, s53, 32768\n"
    "s_waitcnt lgkmcnt(2)\n"
    "v_mfma_f32_16x16x32_bf16 v[16:19], a[0:3], v[80:83], 0\n"
    "ds_read_b64_tr_b16 a[8:9], v144 offset:2048\n"
    "buffer_load_dwordx4 v150, s[8:11], s52 offen lds\n"
    "v_mfma_f32_16x16x32_bf16 v[32:35], a[0:3], v[84:87], 0\n"
    "s_add_u32 s52, s52, 57344\n"
    "ds_read_b64_tr_b16 a[10:11], v144 offset:2560\n"
    "v_mfma_f32_16x16x32_bf16 v[48:51], a[0:3], v[88:91], 0\n"
    "s_add_u32 m0, s53, 36864\n"
    "ds_read_b64_tr_b16 a[12:13], v147 offset:2048\n"
    "v_mfma_f32_16x16x32_bf16 v[64:67], a[0:3], v[92:95], 0\n"
    "s_waitcnt vmcnt(5) lgkmcnt(3)\n"
    "buffer_load_dwordx4 v150, s[8:11], s52 offen lds\n"
    "v_mfma_f32_16x16x32_bf16 v[20:23], a[4:7], v[80:83], 0\n"
    "s_barrier\n"
    "s_add_u32 s52, s52, 57344\n"
    "s_add_u32 m0, s53, 40960\n"
    "ds_read_b64_tr_b16 a[14:15], v147 offset:2560\n"
    "buffer_load_dwordx4 v150, s[8:11], s52 offen lds\n"
    "v_mfma_f32_16x16x32_bf16 v[36:39], a[4:7], v[84:87], 0\n"
    "s_add_u32 s52, s52, 57344\n"
    "ds_read_b64_tr_b16 a[16:17], v144 offset:8192\n"
    "v_mfma_f32_16x16x32_bf16 v[52:55], a[4:7], v[88:91], 0\n"
    "s_add_u32 m0, s53, 45056\n"
    "ds_read_b64_tr_b16 a[18:19], v144 offset:8704\n"
    "v_mfma_f32_16x16x32_bf16 v[68:71], a[4:7], v[92:95], 0\n"
    "s_waitcnt lgkmcnt(4)\n"
    "buffer_load_dwordx4 v150, s[8:11], s52 offen lds\n"
    "v_mfma_f32_16x16x32_bf16 v[24:27], a[8:11], v[80:83], 0\n"
    "s_add_u32 m0, s53, 81920\n"
    "ds_read_b128 v[96:99], v141 offset:16384\n"
    "buffer_load_dwordx4 v178, s[4:7], s26 offen lds\n"
    "v_mfma_f32_16x16x32_bf16 v[40:43], a[8:11], v[84:87], 0\n"
    "ds_read_b128 v[100:103], v141 offset:20480\n"
    "v_mfma_f32_16x16x32_bf16 v[56:59], a[8:11], v[88:91], 0\n"
    "s_add_u32 m0, s53, 86016\n"
    "ds_read_b128 v[104:107], v141 offset:24576\n"
    "v_mfma_f32_16x16x32_bf16 v[72:75], a[8:11], v[92:95], 0\n"
    "s_waitcnt lgkmcnt(5)\n"
    "buffer_load_dwordx4 v179, s[4:7], s26 offen lds\n"
    "v_mfma_f32_16x16x32_bf16 v[28:31], a[12:15], v[80:83], 0\n"
    "s_add_u32 m0, s53, 90112\n"
    "ds_read_b128 v[108:111], v141 offset:28672\n"
    "buffer_load_dwordx4 v180, s[4:7], s26 offen lds\n"
    "v_mfma_f32_16x16x32_bf16 v[44:47], a[12:15], v[84:87], 0\n"
    "ds_read_b64_tr_b16 a[20:21], v147 offset:8192\n"
    "v_mfma_f32_16x16x32_bf16 v[60:63], a[12:15], v[88:91], 0\n"
    "s_add_u32 m0, s53, 94208\n"
    "ds_read_b64_tr_b16 a[22:23], v147 offset:8704\n"
    "v_mfma_f32_16x16x32_bf16 v[76:79], a[12:15], v[92:95], 0\n"
    "s_waitcnt lgkmcnt(3)\n"
    "buffer_load_dwordx4 v181, s[4:7], s26 offen lds\n"
    "v_mfma_f32_16x16x32_bf16 v[16:19], a[16:19], v[96:99], v[16:19]\n"
    "s_waitcnt lgkmcnt(2)\n"
    "ds_read_b64_tr_b16 a[24:25], v144 offset:10240\n"
    "v_mfma_f32_16x16x32_bf16 v[32:35], a[16:19], v[100:103], v[32:35]\n"
    "s_mul_i32 s52, s26, 1792\n"
    "ds_read_b64_tr_b16 a[26:27], v144 offset:10752\n"
    "v_mfma_f32_16x16x32_bf16 v[48:51], a[16:19], v[104:107], v[48:51]\n"
    "s_add_u32 m0, s53, 65536\n"
    "ds_read_b64_tr_b16 a[28:29], v147 offset:10240\n"
    "v_mfma_f32_16x16x32_bf16 v[64:67], a[16:19], v[108:111], v[64:67]\n"
    "s_waitcnt vmcnt(6) lgkmcnt(3)\n"
    "buffer_load_dwordx4 v150, s[8:11], s52 offen lds\n"
    "v_mfma_f32_16x16x32_bf16 v[20:23], a[20:23], v[96:99], v[20:23]\n"
    "s_barrier\n"
    "ds_read_b64_tr_b16 a[30:31], v147 offset:10752\n"
    "v_mfma_f32_16x16x32_bf16 v[36:39], a[20:23], v[100:103], v[36:39]\n"
    "s_add_u32 s52, s52, 57344\n"
    "ds_read_b64_tr_b16 a[32:33], v144 offset:32768\n"
    "v_mfma_f32_16x16x32_bf16 v[52:55], a[20:23], v[104:107], v[52:55]\n"
    "s_add_u32 m0, s53, 69632\n"
    "ds_read_b64_tr_b16 a[34:35], v144 offset:33280\n"
    "v_mfma_f32_16x16x32_bf16 v[68:71], a[20:23], v[108:111], v[68:71]\n"
    "s_waitcnt lgkmcnt(4)\n"
    "buffer_load_dwordx4 v150, s[8:11], s52 offen lds\n"
    "v_mfma_f32_16x16x32_bf16 v[24:27], a[24:27], v[96:99], v[24:27]\n"
    "ds_read_b128 v[80:83], v138 offset:49152\n"
    "v_mfma_f32_16x16x32_bf16 v[40:43], a[24:27], v[100:103], v[40:43]\n"
    "s_add_u32 s52, s52, 57344\n"
    "ds_read_b128 v[84:87], v138 offset:53248\n"
    "v_mfma_f32_16x16x32_bf16 v[56:59], a[24:27], v[104:107], v[56:59]\n"
    "s_add_u32 m0, s53, 73728\n"
    "ds_read_b128 v[88:91], v138 offset:57344\n"
    "v_mfma_f32_16x16x32_bf16 v[72:75], a[24:27], v[108:111], v[72:75]\n"
    "s_waitcnt lgkmcnt(5)\n"
    "buffer_load_dwordx4 v150, s[8:11], s52 offen lds\n"
    "v_mfma_f32_16x16x32_bf16 v[28:31], a[28:31], v[96:99], v[28:31]\n"
    "ds_read_b128 v[92:95], v138 offset:61440\n"
    "v_mfma_f32_16x16x32_bf16 v[44:47], a[28:31], v[100:103], v[44:47]\n"
    "s_add_u32 s52, s52, 57344\n"
    "ds_read_b64_tr_b16 a[36:37], v147 offset:32768\n"
    "v_mfma_f32_16x16x32_bf16 v[60:63], a[28:31], v[104:107], v[60:63]\n"
    "s_add_u32 m0, s53, 77824\n"
    "ds_read_b64_tr_b16 a[38:39], v147 offset:33280\n"
    "v_mfma_f32_16x16x32_bf16 v[76:79], a[28:31], v[108:111], v[76:79]\n"
    "s_waitcnt lgkmcnt(3)\n"
    "buffer_load_dwordx4 v150, s[8:11], s52 offen lds\n"
    "v_mfma_f32_16x16x32_bf16 v[16:19], a[32:35], v[80:83], v[16:19]\n"
    "s_waitcnt lgkmcnt(2)\n"
    "ds_read_b64_tr_b16 a[40:41], v144 offset:34816\n"
    "v_mfma_f32_16x16x32_bf16 v[32:35], a[32:35], v[84:87], v[32:35]\n"
    "ds_read_b64_tr_b16 a[42:43], v144 offset:35328\n"
    "v_mfma_f32_16x16x32_bf16 v[48:51], a[32:35], v[88:91], v[48:51]\n"
    "s_add_u32 m0, s53, 114688\n"
    "ds_read_b64_tr_b16 a[44:45], v147 offset:34816\n"
    "v_mfma_f32_16x16x32_bf16 v[64:67], a[32:35], v[92:95], v[64:67]\n"
    "s_waitcnt vmcnt(8) lgkmcnt(3)\n"
    "buffer_load_dwordx4 v178, s[4:7], s27 offen lds\n"
    "v_mfma_f32_16x16x32_bf16 v[20:23], a[36:39], v[80:83], v[20:23]\n"
    "s_barrier\n"
    "ds_read_b64_tr_b16 a[46:47], v147 offset:35328\n"
    "v_mfma_f32_16x16x32_bf16 v[36:39], a[36:39], v[84:87], v[36:39]\n"
    "ds_read_b64_tr_b16 a[48:49], v144 offset:40960\n"
    "v_mfma_f32_16x16x32_bf16 v[52:55], a[36:39], v[88:91], v[52:55]\n"
    "s_add_u32 m0, s53, 118784\n"
    "ds_read_b64_tr_b16 a[50:51], v144 offset:41472\n"
    "v_mfma_f32_16x16x32_bf16 v[68:71], a[36:39], v[92:95], v[68:71]\n"
    "s_waitcnt lgkmcnt(4)\n"
    "buffer_load_dwordx4 v179, s[4:7], s27 offen lds\n"
    "v_mfma_f32_16x16x32_bf16 v[24:27], a[40:43], v[80:83], v[24:27]\n"
    "ds_read_b128 v[96:99], v141 offset:49152\n"
    "v_mfma_f32_16x16x32_bf16 v[40:43], a[40:43], v[84:87], v[40:43]\n"
    "ds_read_b128 v[100:103], v141 offset:53248\n"
    "v_mfma_f32_16x16x32_bf16 v[56:59], a[40:43], v[88:91], v[56:59]\n"
    "s_add_u32 m0, s53, 122880\n"
    "ds_read_b128 v[104:107], v141 offset:57344\n"
    "v_mfma_f32_16x16x32_bf16 v[72:75], a[40:43], v[92:95], v[72:75]\n"
    "s_waitcnt lgkmcnt(5)\n"
    "buffer_load_dwordx4 v180, s[4:7], s27 offen lds\n"
    "v_mfma_f32_16x16x32_bf16 v[28:31], a[44:47], v[80:83], v[28:31]\n"
    "ds_read_b128 v[108:111], v141 offset:61440\n"
    "v_mfma_f32_16x16x32_bf16 v[44:47], a[44:47], v[84:87], v[44:47]\n"
    "ds_read_b64_tr_b16 a[52:53], v147 offset:40960\n"
    "v_mfma_f32_16x16x32_bf16 v[60:63], a[44:47], v[88:91], v[60:63]\n"
    "s_add_u32 m0, s53, 126976\n"
    "ds_read_b64_tr_b16 a[54:55], v147 offset:41472\n"
    "v_mfma_f32_16x16x32_bf16 v[76:79], a[44:47], v[92:95], v[76:79]\n"
    "s_waitcnt lgkmcnt(3)\n"
    "buffer_load_dwordx4 v181, s[4:7], s27 offen lds\n"
    "v_mfma_f32_16x16x32_bf16 v[16:19], a[48:51], v[96:99], v[16:19]\n"
    "s_waitcnt lgkmcnt(2)\n"
    "ds_read_b64_tr_b16 a[56:57], v144 offset:43008\n"
    "v_mfma_f32_16x16x32_bf16 v[32:35], a[48:51], v[100:103], v[32:35]\n"
    "s_mul_i32 s52, s27, 1792\n"
    "ds_read_b64_tr_b16 a[58:59], v144 offset:43520\n"
    "v_mfma_f32_16x16x32_bf16 v[48:51], a[48:51], v[104:107], v[48:51]\n"
    "s_add_u32 m0, s53, 98304\n"
    "ds_read_b64_tr_b16 a[60:61], v147 offset:43008\n"
    "v_mfma_f32_16x16x32_bf16 v[64:67], a[48:51], v[108:111], v[64:67]\n"
    "s_waitcnt vmcnt(6) lgkmcnt(3)\n"
    "buffer_load_dwordx4 v150, s[8:11], s52 offen lds\n"
    "v_mfma_f32_16x16x32_bf16 v[20:23], a[52:55], v[96:99], v[20:23]\n"
    "s_barrier\n"
    "ds_read_b64_tr_b16 a[62:63], v147 offset:43520\n"
    "v_mfma_f32_16x16x32_bf16 v[36:39], a[52:55], v[100:103], v[36:39]\n"
    "s_add_u32 s52, s52, 57344\n"
    "ds_read_b64_tr_b16 a[64:65], v145 offset:0\n"
    "v_mfma_f32_16x16x32_bf16 v[52:55], a[52:55], v[104:107], v[52:55]\n"
    "s_add_u32 m0, s53, 102400\n"
    "ds_read_b64_tr_b16 a[66:67], v145 offset:512\n"
    "v_mfma_f32_16x16x32_bf16 v[68:71], a[52:55], v[108:111], v[68:71]\n"
    "s_waitcnt lgkmcnt(4)\n"
    "buffer_load_dwordx4 v150, s[8:11], s52 offen lds\n"
    "v_mfma_f32_16x16x32_bf16 v[24:27], a[56:59], v[96:99], v[24:27]\n"
    "ds_read_b128 v[80:83], v139 offset:16384\n"
    "v_mfma_f32_16x16x32_bf16 v[40:43], a[56:59], v[100:103], v[40:43]\n"
    "s_add_u32 s52, s52, 57344\n"
    "ds_read_b128 v[84:87], v139 offset:20480\n"
    "v_mfma_f32_16x16x32_bf16 v[56:59], a[56:59], v[104:107], v[56:59]\n"
    "s_add_u32 m0, s53, 106496\n"
    "ds_read_b128 v[88:91], v139 offset:24576\n"
    "v_mfma_f32_16x16x32_bf16 v[72:75], a[56:59], v[108:111], v[72:75]\n"
    "s_waitcnt lgkmcnt(5)\n"
    "buffer_load_dwordx4 v150, s[8:11], s52 offen lds\n"
    "v_mfma_f32_16x16x32_bf16 v[28:31], a[60:63], v[96:99], v[28:31]\n"
    "ds_read_b128 v[92:95], v139 offset:28672\n"
    "v_mfma_f32_16x16x32_bf16 v[44:47], a[60:63], v[100:103], v[44:47]\n"
    "s_add_u32 s52, s52, 57344\n"
    "ds_read_b64_tr_b16 a[68:69], v148 offset:0\n"
    "v_mfma_f32_16x16x32_bf16 v[60:63], a[60:63], v[104:107], v[60:63]\n"
    "s_add_u32 m0, s53, 110592\n"
    "ds_read_b64_tr_b16 a[70:71], v148 offset:512\n"
    "v_mfma_f32_16x16x32_bf16 v[76:79], a[60:63], v[108:111], v[76:79]\n"
    "s_waitcnt lgkmcnt(3)\n"
    "buffer_load_dwordx4 v150, s[8:11], s52 offen lds\n"
    "v_mfma_f32_16x16x32_bf16 v[16:19], a[64:67], v[80:83], v[16:19]\n"
    "s_waitcnt lgkmcnt(2)\n"
    "ds_read_b64_tr_b16 a[72:73], v145 offset:2048\n"
    "v_mfma_f32_16x16x32_bf16 v[32:35], a[64:67], v[84:87], v[32:35]\n"
    "ds_read_b64_tr_b16 a[74:75], v145 offset:2560\n"
    "v_mfma_f32_16x16x32_bf16 v[48:51], a[64:67], v[88:91], v[48:51]\n"
    "s_add_u32 m0, s53, 147456\n"
    "ds_read_b64_tr_b16 a[76:77], v148 offset:2048\n"
    "v_mfma_f32_16x16x32_bf16 v[64:67], a[64:67], v[92:95], v[64:67]\n"
    "s_waitcnt vmcnt(8) lgkmcnt(3)\n"
    "buffer_load_dwordx4 v178, s[4:7], s28 offen lds\n"
    "v_mfma_f32_16x16x32_bf16 v[20:23], a[68:71], v[80:83], v[20:23]\n"
    "s_barrier\n"
    "ds_read_b64_tr_b16 a[78:79], v148 offset:2560\n"
    "v_mfma_f32_16x16x32_bf16 v[36:39], a[68:71], v[84:87], v[36:39]\n"
    "ds_read_b64_tr_b16 a[80:81], v145 offset:8192\n"
    "v_mfma_f32_16x16x32_bf16 v[52:55], a[68:71], v[88:91], v[52:55]\n"
    "s_add_u32 m0, s53, 151552\n"
    "ds_read_b64_tr_b16 a[82:83], v145 offset:8704\n"
    "v_mfma_f32_16x16x32_bf16 v[68:71], a[68:71], v[92:95], v[68:71]\n"
    "s_waitcnt lgkmcnt(4)\n"
    "buffer_load_dwordx4 v179, s[4:7], s28 offen lds\n"
    "v_mfma_f32_16x16x32_bf16 v[24:27], a[72:75], v[80:83], v[24:27]\n"
    "ds_read_b128 v[96:99], v142 offset:16384\n"
    "v_mfma_f32_16x16x32_bf16 v[40:43], a[72:75], v[84:87], v[40:43]\n"
    "ds_read_b128 v[100:103], v142 offset:20480\n"
    "v_mfma_f32_16x16x32_bf16 v[56:59], a[72:75], v[88:91], v[56:59]\n"
    "s_add_u32 m0, s53, 155648\n"
    "ds_read_b128 v[104:107], v142 offset:24576\n"
    "v_mfma_f32_16x16x32_bf16 v[72:75], a[72:75], v[92:95], v[72:75]\n"
    "s_waitcnt lgkmcnt(5)\n"
    "buffer_load_dwordx4 v180, s[4:7], s28 offen lds\n"
    "v_mfma_f32_16x16x32_bf16 v[28:31], a[76:79], v[80:83], v[28:31]\n"
    "ds_read_b128 v[108:111], v142 offset:28672\n"
    "v_mfma_f32_16x16x32_bf16 v[44:47], a[76:79], v[84:87], v[44:47]\n"
    "ds_read_b64_tr_b16 a[84:85], v148 offset:8192\n"
    "v_mfma_f32_16x16x32_bf16 v[60:63], a[76:79], v[88:91], v[60:63]\n"
    "s_add_u32 m0, s53, 159744\n"
    "ds_read_b64_tr_b16 a[86:87], v148 offset:8704\n"
    "v_mfma_f32_16x16x32_bf16 v[76:79], a[76:79], v[92:95], v[76:79]\n"
    "s_waitcnt lgkmcnt(3)\n"
    "buffer_load_dwordx4 v181, s[4:7], s28 offen lds\n"
    "v_mfma_f32_16x16x32_bf16 v[16:19], a[80:83], v[96:99], v[16:19]\n"
    "s_waitcnt lgkmcnt(2)\n"
    "ds_read_b64_tr_b16 a[88:89], v145 offset:10240\n"
    "v_mfma_f32_16x16x32_bf16 v[32:35], a[80:83], v[100:103], v[32:35]\n"
    "s_mul_i32 s52, s28, 1792\n"
    "ds_read_b64_tr_b16 a[90:91], v145 offset:10752\n"
    "v_mfma_f32_16x16x32_bf16 v[48:51], a[80:83], v[104:107], v[48:51]\n"
    "s_add_u32 m0, s53, 131072\n"
    "ds_read_b64_tr_b16 a[92:93], v148 offset:10240\n"
    "v_mfma_f32_16x16x32_bf16 v[64:67], a[80:83], v[108:111], v[64:67]\n"
    "s_waitcnt vmcnt(6) lgkmcnt(3)\n"
    "buffer_load_dwordx4 v150, s[8:11], s52 offen lds\n"
    "v_mfma_f32_16x16x32_bf16 v[20:23], a[84:87], v[96:99], v[20:23]\n"
    "s_barrier\n"
    "ds_read_b64_tr_b16 a[94:95], v148 offset:10752\n"
    "v_mfma_f32_16x16x32_bf16 v[36:39], a[84:87], v[100:103], v[36:39]\n"
    "s_add_u32 s52, s52, 57344\n"
    "ds_read_b64_tr_b16 a[96:97], v145 offset:32768\n"
    "v_mfma_f32_16x16x32_bf16 v[52:55], a[84:87], v[104:107], v[52:55]\n"
    "s_add_u32 m0, s53, 135168\n"
    "ds_read_b64_tr_b16 a[98:99], v145 offset:33280\n"
    "v_mfma_f32_16x16x32_bf16 v[68:71], a[84:87], v[108:111], v[68:71]\n"
    "s_waitcnt lgkmcnt(4)\n"
    "buffer_load_dwordx4 v150, s[8:11], s52 offen lds\n"
    "v_mfma_f32_16x16x32_bf16 v[24:27], a[88:91], v[96:99], v[24:27]\n"
    "ds_read_b128 v[80:83], v139 offset:49152\n"
    "v_mfma_f32_16x16x32_bf16 v[40:43], a[88:91], v[100:103], v[40:43]\n"
    "s_add_u32 s52, s52, 57344\n"
    "ds_read_b128 v[84:87], v139 offset:53248\n"
    "v_mfma_f32_16x16x32_bf16 v[56:59], a[88:91], v[104:107], v[56:59]\n"
    "s_add_u32 m0, s53, 139264\n"
    "ds_read_b128 v[88:91], v139 offset:57344\n"
    "L_stub0:\n"
    "s_cbranch_execz L_warm_back\n"
    "v_mfma_f32_16x16x32_bf16 v[72:75], a[88:91], v[108:111], v[72:75]\n"
    "s_waitcnt lgkmcnt(5)\n"
    "buffer_load_dwordx4 v150, s[8:11], s52 offen lds\n"
    "v_mfma_f32_16x16x32_bf16 v[28:31], a[92:95], v[96:99], v[28:31]\n"
    "ds_read_b128 v[92:95], v139 offset:61440\n"
    "v_mfma_f32_16x16x32_bf16 v[44:47], a[92:95], v[100:103], v[44:47]\n"
    "s_add_u32 s52, s52, 57344\n"
    "ds_read_b64_tr_b16 a[100:101], v148 offset:32768\n"
    "v_mfma_f32_16x16x32_bf16 v[60:63], a[92:95], v[104:107], v[60:63]\n"
    "s_add_u32 m0, s53, 143360\n"
    "ds_read_b64_tr_b16 a[102:103], v148 offset:33280\n"
    "v_mfma_f32_16x16x32_bf16 v[76:79], a[92:95], v[108:111], v[76:79]\n"
    "s_waitcnt lgkmcnt(3)\n"
    "buffer_load_dwordx4 v150, s[8:11], s52 offen lds\n"
    "v_mfma_f32_16x16x32_bf16 v[16:19], a[96:99], v[80:83], v[16:19]\n"
    "s_waitcnt lgkmcnt(2)\n"
    "ds_read_b64_tr_b16 a[104:105], v145 offset:34816\n"
    "v_mfma_f32_16x16x32_bf16 v[32:35], a[96:99], v[84:87], v[32:35]\n"
    "ds_read_b64_tr_b16 a[106:107], v145 offset:35328\n"
    "v_mfma_f32_16x16x32_bf16 v[48:51], a[96:99], v[88:91], v[48:51]\n"
    "s_add_u32 m0, s53, 16384\n"
    "ds_read_b64_tr_b16 a[108:109], v148 offset:34816\n"
    "v_mfma_f32_16x16x32_bf16 v[64:67], a[96:99], v[92:95], v[64:67]\n"
    "s_waitcnt vmcnt(8) lgkmcnt(3)\n"
    "buffer_load_dwordx4 v178, s[4:7], s29 offen lds\n"
    "v_mfma_f32_16x16x32_bf16 v[20:23], a[100:103], v[80:83], v[20:23]\n"
    "s_barrier\n"
    "ds_read_b64_tr_b16 a[110:111], v148 offset:35328\n"
    "v_mfma_f32_16x16x32_bf16 v[36:39], a[100:103], v[84:87], v[36:39]\n"
    "ds_read_b64_tr_b16 a[112:113], v145 offset:40960\n"
    "v_mfma_f32_16x16x32_bf16 v[52:55], a[100:103], v[88:91], v[52:55]\n"
    "s_add_u32 m0, s53, 20480\n"
    "ds_read_b64_tr_b16 a[114:115], v145 offset:41472\n"
    "v_mfma_f32_16x16x32_bf16 v[68:71], a[100:103], v[92:95], v[68:71]\n"
    "s_waitcnt lgkmcnt(4)\n"
    "buffer_load_dwordx4 v179, s[4:7], s29 offen lds\n"
    "v_mfma_f32_16x16x32_bf16 v[24:27], a[104:107], v[80:83], v[24:27]\n"
    "ds_read_b128 v[96:99], v142 offset:49152\n"
    "v_mfma_f32_16x16x32_bf16 v[40:43], a[104:107], v[84:87], v[40:43]\n"
    "ds_read_b128 v[100:103], v142 offset:53248\n"
    "v_mfma_f32_16x16x32_bf16 v[56:59], a[104:107], v[88:91], v[56:59]\n"
    "s_add_u32 m0, s53, 24576\n"
    "ds_read_b128 v[104:107], v142 offset:57344\n"
    "v_mfma_f32_16x16x32_bf16 v[72:75], a[104:107], v[92:95], v[72:75]\n"
    "s_waitcnt lgkmcnt(5)\n"
    "buffer_load_dwordx4 v180, s[4:7], s29 offen lds\n"
    "v_mfma_f32_16x16x32_bf16 v[28:31], a[108:111], v[80:83], v[28:31]\n"
    "ds_read_b128 v[108:111], v142 offset:61440\n"
    "v_mfma_f32_16x16x32_bf16 v[44:47], a[108:111], v[84:87], v[44:47]\n"
    "ds_read_b64_tr_b16 a[116:117], v148 offset:40960\n"
    "v_mfma_f32_16x16x32_bf16 v[60:63], a[108:111], v[88:91], v[60:63]\n"
    "s_add_u32 m0, s53, 28672\n"
    "ds_read_b64_tr_b16 a[118:119], v148 offset:41472\n"
    "v_mfma_f32_16x16x32_bf16 v[76:79], a[108:111], v[92:95], v[76:79]\n"
    "s_waitcnt lgkmcnt(3)\n"
    "buffer_load_dwordx4 v181, s[4:7], s29 offen lds\n"
    "v_mfma_f32_16x16x32_bf16 v[16:19], a[112:115], v[96:99], v[16:19]\n"
    "s_waitcnt lgkmcnt(2)\n"
    "ds_read_b64_tr_b16 a[120:121], v145 offset:43008\n"
    "v_mfma_f32_16x16x32_bf16 v[32:35], a[112:115], v[100:103], v[32:35]\n"
    "s_mul_i32 s52, s29, 1792\n"
    "ds_read_b64_tr_b16 a[122:123], v145 offset:43520\n"
    "v_mfma_f32_16x16x32_bf16 v[48:51], a[112:115], v[104:107], v[48:51]\n"
    "s_add_u32 m0, s53, 0\n"
    "ds_read_b64_tr_b16 a[124:125], v148 offset:43008\n"
    "v_mfma_f32_16x16x32_bf16 v[64:67], a[112:115], v[108:111], v[64:67]\n"
    "s_waitcnt vmcnt(6) lgkmcnt(3)\n"
    "buffer_load_dwordx4 v150, s[8:11], s52 offen lds\n"
    "v_mfma_f32_16x16x32_bf16 v[20:23], a[116:119], v[96:99], v[20:23]\n"
    "s_barrier\n"
    "ds_read_b64_tr_b16 a[126:127], v148 offset:43520\n"
    "v_mfma_f32_16x16x32_bf16 v[36:39], a[116:119], v[100:103], v[36:39]\n"
    "s_add_u32 s52, s52, 57344\n"
    "ds_read_b64_tr_b16 a[128:129], v146 offset:0\n"
    "v_mfma_f32_16x16x32_bf16 v[52:55], a[116:119], v[104:107], v[52:55]\n"
    "s_add_u32 m0, s53, 4096\n"
    "ds_read_b64_tr_b16 a[130:131], v146 offset:512\n"
    "v_mfma_f32_16x16x32_bf16 v[68:71], a[116:119], v[108:111], v[68:71]\n"
    "s_waitcnt lgkmcnt(4)\n"
    "buffer_load_dwordx4 v150, s[8:11], s52 offen lds\n"
    "v_mfma_f32_16x16x32_bf16 v[24:27], a[120:123], v[96:99], v[24:27]\n"
    "ds_read_b128 v[80:83], v140 offset:16384\n"
    "v_mfma_f32_16x16x32_bf16 v[40:43], a[120:123], v[100:103], v[40:43]\n"
    "s_add_u32 s52, s52, 57344\n"
    "ds_read_b128 v[84:87], v140 offset:20480\n"
    "v_mfma_f32_16x16x32_bf16 v[56:59], a[120:123], v[104:107], v[56:59]\n"
    "s_add_u32 m0, s53, 8192\n"
    "ds_read_b128 v[88:91], v140 offset:24576\n"
    "v_mfma_f32_16x16x32_bf16 v[72:75], a[120:123], v[108:111], v[72:75]\n"
    "s_waitcnt lgkmcnt(5)\n"
    "buffer_load_dwordx4 v150, s[8:11], s52 offen lds\n"
    "v_mfma_f32_16x16x32_bf16 v[28:31], a[124:127], v[96:99], v[28:31]\n"
    "ds_read_b128 v[92:95], v140 offset:28672\n"
    "v_mfma_f32_16x16x32_bf16 v[44:47], a[124:127], v[100:103], v[44:47]\n"
    "s_add_u32 s52, s52, 57344\n"
    "ds_read_b64_tr_b16 a[132:133], v149 offset:0\n"
    "v_mfma_f32_16x16x32_bf16 v[60:63], a[124:127], v[104:107], v[60:63]\n"
    "s_add_u32 m0, s53, 12288\n"
    "ds_read_b64_tr_b16 a[134:135], v149 offset:512\n"
    "v_mfma_f32_16x16x32_bf16 v[76:79], a[124:127], v[108:111], v[76:79]\n"
    "s_waitcnt lgkmcnt(3)\n"
    "buffer_load_dwordx4 v150, s[8:11], s52 offen lds\n"
    "v_mfma_f32_16x16x32_bf16 v[16:19], a[128:131], v[80:83], v[16:19]\n"
    "s_waitcnt lgkmcnt(2)\n"
    "ds_read_b64_tr_b16 a[136:137], v146 offset:2048\n"
    "v_mfma_f32_16x16x32_bf16 v[32:35], a[128:131], v[84:87], v[32:35]\n"
    "ds_read_b64_tr_b16 a[138:139], v146 offset:2560\n"
    "v_mfma_f32_16x16x32_bf16 v[48:51], a[128:131], v[88:91], v[48:51]\n"
    "s_add_u32 m0, s53, 49152\n"
    "ds_read_b64_tr_b16 a[140:141], v149 offset:2048\n"
    "v_mfma_f32_16x16x32_bf16 v[64:67], a[128:131], v[92:95], v[64:67]\n"
    "s_waitcnt vmcnt(8) lgkmcnt(3)\n"
    "buffer_load_dwordx4 v178, s[4:7], s30 offen lds\n"
    "v_mfma_f32_16x16x32_bf16 v[20:23], a[132:135], v[80:83], v[20:23]\n"
    "s_barrier\n"
    "ds_read_b64_tr_b16 a[142:143], v149 offset:2560\n"
    "v_mfma_f32_16x16x32_bf16 v[36:39], a[132:135], v[84:87], v[36:39]\n"
    "ds_read_b64_tr_b16 a[144:145], v146 offset:8192\n"
    "v_mfma_f32_16x16x32_bf16 v[52:55], a[132:135], v[88:91], v[52:55]\n"
    "s_add_u32 m0, s53, 53248\n"
    "ds_read_b64_tr_b16 a[146:147], v146 offset:8704\n"
    "v_mfma_f32_16x16x32_bf16 v[68:71], a[132:135], v[92:95], v[68:71]\n"
    "s_waitcnt lgkmcnt(4)\n"
    "buffer_load_dwordx4 v179, s[4:7], s30 offen lds\n"
    "v_mfma_f32_16x16x32_bf16 v[24:27], a[136:139], v[80:83], v[24:27]\n"
    "ds_read_b128 v[96:99], v143 offset:16384\n"
    "v_mfma_f32_16x16x32_bf16 v[40:43], a[136:139], v[84:87], v[40:43]\n"
    "ds_read_b128 v[100:103], v143 offset:20480\n"
    "v_mfma_f32_16x16x32_bf16 v[56:59], a[136:139], v[88:91], v[56:59]\n"
    "s_add_u32 m0, s53, 57344\n"
    "ds_read_b128 v[104:107], v143 offset:24576\n"
    "v_mfma_f32_16x16x32_bf16 v[72:75], a[136:139], v[92:95], v[72:75]\n"
    "s_waitcnt lgkmcnt(5)\n"
    "buffer_load_dwordx4 v180, s[4:7], s30 offen lds\n"
    "v_mfma_f32_16x16x32_bf16 v[28:31], a[140:143], v[80:83], v[28:31]\n"
    "ds_read_b128 v[108:111], v143 offset:28672\n"
    "v_mfma_f32_16x16x32_bf16 v[44:47], a[140:143], v[84:87], v[44:47]\n"
    "ds_read_b64_tr_b16 a[148:149], v149 offset:8192\n"
    "v_mfma_f32_16x16x32_bf16 v[60:63], a[140:143], v[88:91], v[60:63]\n"
    "s_add_u32 m0, s53, 61440\n"
    "ds_read_b64_tr_b16 a[150:151], v149 offset:8704\n"
    "v_mfma_f32_16x16x32_bf16 v[76:79], a[140:143], v[92:95], v[76:79]\n"
    "s_waitcnt lgkmcnt(3)\n"
    "buffer_load_dwordx4 v181, s[4:7], s30 offen lds\n"
    "v_mfma_f32_16x16x32_bf16 v[16:19], a[144:147], v[96:99], v[16:19]\n"
    "s_waitcnt lgkmcnt(2)\n"
    "ds_read_b64_tr_b16 a[152:153], v146 offset:10240\n"
    "v_mfma_f32_16x16x32_bf16 v[32:35], a[144:147], v[100:103], v[32:35]\n"
    "s_mul_i32 s52, s30, 1792\n"
    "ds_read_b64_tr_b16 a[154:155], v146 offset:10752\n"
    "v_mfma_f32_16x16x32_bf16 v[48:51], a[144:147], v[104:107], v[48:51]\n"
    "s_add_u32 m0, s53, 32768\n"
    "ds_read_b64_tr_b16 a[156:157], v149 offset:10240\n"
    "v_mfma_f32_16x16x32_bf16 v[64:67], a[144:147], v[108:111], v[64:67]\n"
    "s_waitcnt vmcnt(6) lgkmcnt(3)\n"
    "buffer_load_dwordx4 v150, s[8:11], s52 offen lds\n"
    "v_mfma_f32_16x16x32_bf16 v[20:23], a[148:151], v[96:99], v[20:23]\n"
    "s_barrier\n"
    "ds_read_b64_tr_b16 a[158:159], v149 offset:10752\n"
    "v_mfma_f32_16x16x32_bf16 v[36:39], a[148:151], v[100:103], v[36:39]\n"
    "s_add_u32 s52, s52, 57344\n"
    "ds_read_b64_tr_b16 a[160:161], v144 offset:0\n"
    "v_mfma_f32_16x16x32_bf16 v[52:55], a[148:151], v[104:107], v[52:55]\n"
    "s_add_u32 m0, s53, 36864\n"
    "ds_read_b64_tr_b16 a[162:163], v144 offset:512\n"
    "v_mfma_f32_16x16x32_bf16 v[68:71], a[148:151], v[108:111], v[68:71]\n"
    "s_waitcnt lgkmcnt(4)\n"
    "buffer_load_dwordx4 v150, s[8:11], s52 offen lds\n"
    "v_mfma_f32_16x16x32_bf16 v[24:27], a[152:155], v[96:99], v[24:27]\n"
    "ds_read_b128 v[80:83], v138 offset:16384\n"
    "v_mfma_f32_16x16x32_bf16 v[40:43], a[152:155], v[100:103], v[40:43]\n"
    "s_add_u32 s52, s52, 57344\n"
    "ds_read_b128 v[84:87], v138 offset:20480\n"
    "v_mfma_f32_16x16x32_bf16 v[56:59], a[152:155], v[104:107], v[56:59]\n"
    "s_add_u32 m0, s53, 40960\n"
    "ds_read_b128 v[88:91], v138 offset:24576\n"
    "v_mfma_f32_16x16x32_bf16 v[72:75], a[152:155], v[108:111], v[72:75]\n"
    "s_waitcnt lgkmcnt(5)\n"
    "buffer_load_dwordx4 v150, s[8:11], s52 offen lds\n"
    "v_mfma_f32_16x16x32_bf16 v[28:31], a[156:159], v[96:99], v[28:31]\n"
    "ds_read_b128 v[92:95], v138 offset:28672\n"
    "v_mfma_f32_16x16x32_bf16 v[44:47], a[156:159], v[100:103], v[44:47]\n"
    "s_add_u32 s52, s52, 57344\n"
    "ds_read_b64_tr_b16 a[164:165], v147 offset:0\n"
    "v_mfma_f32_16x16x32_bf16 v[60:63], a[156:159], v[104:107], v[60:63]\n"
    "s_add_u32 m0, s53, 45056\n"
    "ds_read_b64_tr_b16 a[166:167], v147 offset:512\n"
    "v_mfma_f32_16x16x32_bf16 v[76:79], a[156:159], v[108:111], v[76:79]\n"
    "s_waitcnt lgkmcnt(3)\n"
    "buffer_load_dwordx4 v150, s[8:11], s52 offen lds\n"
    "v_mfma_f32_16x16x32_bf16 v[16:19], a[160:163], v[80:83], v[16:19]\n"
    "s_waitcnt lgkmcnt(2)\n"
    "ds_read_b64_tr_b16 a[168:169], v144 offset:2048\n"
    "v_mfma_f32_16x16x32_bf16 v[32:35], a[160:163], v[84:87], v[32:35]\n"
    "ds_read_b64_tr_b16 a[170:171], v144 offset:2560\n"
    "v_mfma_f32_16x16x32_bf16 v[48:51], a[160:163], v[88:91], v[48:51]\n"
    "s_add_u32 m0, s53, 81920\n"
    "ds_read_b64_tr_b16 a[172:173], v147 offset:2048\n"
    "v_mfma_f32_16x16x32_bf16 v[64:67], a[160:163], v[92:95], v[64:67]\n"
    "s_waitcnt vmcnt(8) lgkmcnt(3)\n"
    "buffer_load_dwordx4 v178, s[4:7], s31 offen lds\n"
    "v_mfma_f32_16x16x32_bf16 v[20:23], a[164:167], v[80:83], v[20:23]\n"
    "s_barrier\n"
    "ds_read_b64_tr_b16 a[174:175], v147 offset:2560\n"
    "v_mfma_f32_16x16x32_bf16 v[36:39], a[164:167], v[84:87], v[36:39]\n"
    "ds_read_b64_tr_b16 a[176:177], v144 offset:8192\n"
    "v_mfma_f32_16x16x32_bf16 v[52:55], a[164:167], v[88:91], v[52:55]\n"
    "s_add_u32 m0, s53, 86016\n"
    "ds_read_b64_tr_b16 a[178:179], v144 offset:8704\n"
    "v_mfma_f32_16x16x32_bf16 v[68:71], a[164:167], v[92:95], v[68:71]\n"
    "s_waitcnt lgkmcnt(4)\n"
    "buffer_load_dwordx4 v179, s[4:7], s31 offen lds\n"
    "v_mfma_f32_16x16x32_bf16 v[24:27], a[168:171], v[80:83], v[24:27]\n"
    "ds_read_b128 v[96:99], v141 offset:16384\n"
    "v_mfma_f32_16x16x32_bf16 v[40:43], a[168:171], v[84:87], v[40:43]\n"
    "ds_read_b128 v[100:103], v141 offset:20480\n"
    "v_mfma_f32_16x16x32_bf16 v[56:59], a[168:171], v[88:91], v[56:59]\n"
    "s_add_u32 m0, s53, 90112\n"
    "ds_read_b128 v[104:107], v141 offset:24576\n"
    "v_mfma_f32_16x16x32_bf16 v[72:75], a[168:171], v[92:95], v[72:75]\n"
    "s_waitcnt lgkmcnt(5)\n"
    "buffer_load_dwordx4 v180, s[4:7], s31 offen lds\n"
    "v_mfma_f32_16x16x32_bf16 v[28:31], a[172:175], v[80:83], v[28:31]\n"
    "ds_read_b128 v[108:111], v141 offset:28672\n"
    "v_mfma_f32_16x16x32_bf16 v[44:47], a[172:175], v[84:87], v[44:47]\n"
    "ds_read_b64_tr_b16 a[180:181], v147 offset:8192\n"
    "v_mfma_f32_16x16x32_bf16 v[60:63], a[172:175], v[88:91], v[60:63]\n"
    "s_add_u32 m0, s53, 94208\n"
    "ds_read_b64_tr_b16 a[182:183], v147 offset:8704\n"
    "v_mfma_f32_16x16x32_bf16 v[76:79], a[172:175], v[92:95], v[76:79]\n"
    "s_waitcnt lgkmcnt(3)\n"
    "buffer_load_dwordx4 v181, s[4:7], s31 offen lds\n"
    "v_mfma_f32_16x16x32_bf16 v[16:19], a[176:179], v[96:99], v[16:19]\n"
    "s_waitcnt lgkmcnt(2)\n"
    "ds_read_b64_tr_b16 a[184:185], v144 offset:10240\n"
    "v_mfma_f32_16x16x32_bf16 v[32:35], a[176:179], v[100:103], v[32:35]\n"
    "s_mul_i32 s52, s31, 1792\n"
    "ds_read_b64_tr_b16 a[186:187], v144 offset:10752\n"
    "v_mfma_f32_16x16x32_bf16 v[48:51], a[176:179], v[104:107], v[48:51]\n"
    "s_add_u32 m0, s53, 65536\n"
    "ds_read_b64_tr_b16 a[188:189], v147 offset:10240\n"
    "v_mfma_f32_16x16x32_bf16 v[64:67], a[176:179], v[108:111], v[64:67]\n"
    "s_waitcnt vmcnt(6) lgkmcnt(3)\n"
    "buffer_load_dwordx4 v150, s[8:11], s52 offen lds\n"
    "v_mfma_f32_16x16x32_bf16 v[20:23], a[180:183], v[96:99], v[20:23]\n"
    "s_barrier\n"
    "ds_read_b64_tr_b16 a[190:191], v147 offset:10752\n"
    "v_mfma_f32_16x16x32_bf16 v[36:39], a[180:183], v[100:103], v[36:39]\n"
    "s_add_u32 s52, s52, 57344\n"
    "ds_read_b64_tr_b16 a[192:193], v144 offset:32768\n"
    "v_mfma_f32_16x16x32_bf16 v[52:55], a[180:183], v[104:107], v[52:55]\n"
    "s_add_u32 m0, s53, 69632\n"
    "ds_read_b64_tr_b16 a[194:195], v144 offset:33280\n"
    "v_mfma_f32_16x16x32_bf16 v[68:71], a[180:183], v[108:111], v[68:71]\n"
    "s_waitcnt lgkmcnt(4)\n"
    "buffer_load_dwordx4 v150, s[8:11], s52 offen lds\n"
    "v_mfma_f32_16x16x32_bf16 v[24:27], a[184:187], v[96:99], v[24:27]\n"
    "ds_read_b128 v[80:83], v138 offset:49152\n"
    "v_mfma_f32_16x16x32_bf16 v[40:43], a[184:187], v[100:103], v[40:43]\n"
    "s_add_u32 s52, s52, 57344\n"
    "ds_read_b128 v[84:87], v138 offset:53248\n"
    "v_mfma_f32_16x16x32_bf16 v[56:59], a[184:187], v[104:107], v[56:59]\n"
    "s_add_u32 m0, s53, 73728\n"
    "ds_read_b128 v[88:91], v138 offset:57344\n"
    "v_mfma_f32_16x16x32_bf16 v[72:75], a[184:187], v[108:111], v[72:75]\n"
    "s_waitcnt lgkmcnt(5)\n"
    "buffer_load_dwordx4 v150, s[8:11], s52 offen lds\n"
    "v_mfma_f32_16x16x32_bf16 v[28:31], a[188:191], v[96:99], v[28:31]\n"
    "ds_read_b128 v[92:95], v138 offset:61440\n"
    "v_mfma_f32_16x16x32_bf16 v[44:47], a[188:191], v[100:103], v[44:47]\n"
    "s_add_u32 s52, s52, 57344\n"
    "ds_read_b64_tr_b16 a[196:197], v147 offset:32768\n"
    "v_mfma_f32_16x16x32_bf16 v[60:63], a[188:191], v[104:107], v[60:63]\n"
    "s_add_u32 m0, s53, 77824\n"
    "ds_read_b64_tr_b16 a[198:199], v147 offset:33280\n"
    "v_mfma_f32_16x16x32_bf16 v[76:79], a[188:191], v[108:111], v[76:79]\n"
    "s_waitcnt lgkmcnt(3)\n"
    "buffer_load_dwordx4 v150, s[8:11], s52 offen lds\n"
    "v_mfma_f32_16x16x32_bf16 v[16:19], a[192:195], v[80:83], v[16:19]\n"
    "s_waitcnt lgkmcnt(2)\n"
    "ds_read_b64_tr_b16 a[200:201], v144 offset:34816\n"
    "v_mfma_f32_16x16x32_bf16 v[32:35], a[192:195], v[84:87], v[32:35]\n"
    "ds_read_b64_tr_b16 a[202:203], v144 offset:35328\n"
    "v_mfma_f32_16x16x32_bf16 v[48:51], a[192:195], v[88:91], v[48:51]\n"
    "s_add_u32 m0, s53, 98304\n"
    "ds_read_b64_tr_b16 a[204:205], v147 offset:34816\n"
    "v_mfma_f32_16x16x32_bf16 v[64:67], a[192:195], v[92:95], v[64:67]\n"
    "s_waitcnt vmcnt(8) lgkmcnt(3)\n"
    "buffer_load_dwordx4 v182, s[4:7], s24 offen lds\n"
    "v_mfma_f32_16x16x32_bf16 v[20:23], a[196:199], v[80:83], v[20:23]\n"
    "s_barrier\n"
    "ds_read_b64_tr_b16 a[206:207], v147 offset:35328\n"
    "v_mfma_f32_16x16x32_bf16 v[36:39], a[196:199], v[84:87], v[36:39]\n"
    "ds_read_b64_tr_b16 a[208:209], v144 offset:40960\n"
    "v_mfma_f32_16x16x32_bf16 v[52:55], a[196:199], v[88:91], v[52:55]\n"
    "s_add_u32 m0, s53, 102400\n"
    "ds_read_b64_tr_b16 a[210:211], v144 offset:41472\n"
    "v_mfma_f32_16x16x32_bf16 v[68:71], a[196:199], v[92:95], v[68:71]\n"
    "s_waitcnt lgkmcnt(4)\n"
    "buffer_load_dwordx4 v182, s[4:7], s25 offen lds\n"
    "v_mfma_f32_16x16x32_bf16 v[24:27], a[200:203], v[80:83], v[24:27]\n"
    "ds_read_b128 v[96:99], v141 offset:49152\n"
    "v_mfma_f32_16x16x32_bf16 v[40:43], a[200:203], v[84:87], v[40:43]\n"
    "ds_read_b128 v[100:103], v141 offset:53248\n"
    "v_mfma_f32_16x16x32_bf16 v[56:59], a[200:203], v[88:91], v[56:59]\n"
    "s_add_u32 m0, s53, 106496\n"
    "ds_read_b128 v[104:107], v141 offset:57344\n"
    "v_mfma_f32_16x16x32_bf16 v[72:75], a[200:203], v[92:95], v[72:75]\n"
    "s_waitcnt lgkmcnt(5)\n"
    "buffer_load_dwordx4 v182, s[4:7], s26 offen lds\n"
    "v_mfma_f32_16x16x32_bf16 v[28:31], a[204:207], v[80:83], v[28:31]\n"
    "ds_read_b128 v[108:111], v141 offset:61440\n"
    "v_mfma_f32_16x16x32_bf16 v[44:47], a[204:207], v[84:87], v[44:47]\n"
    "ds_read_b64_tr_b16 a[212:213], v147 offset:40960\n"
    "v_mfma_f32_16x16x32_bf16 v[60:63], a[204:207], v[88:91], v[60:63]\n"
    "s_add_u32 m0, s53, 110592\n"
    "ds_read_b64_tr_b16 a[214:215], v147 offset:41472\n"
    "v_mfma_f32_16x16x32_bf16 v[76:79], a[204:207], v[92:95], v[76:79]\n"
    "s_waitcnt lgkmcnt(3)\n"
    "buffer_load_dwordx4 v182, s[4:7], s27 offen lds\n"
    "v_mfma_f32_16x16x32_bf16 v[16:19], a[208:211], v[96:99], v[16:19]\n"
    "s_waitcnt lgkmcnt(2)\n"
    "ds_read_b64_tr_b16 a[216:217], v144 offset:43008\n"
    "v_mfma_f32_16x16x32_bf16 v[32:35], a[208:211], v[100:103], v[32:35]\n"
    "ds_read_b64_tr_b16 a[218:219], v144 offset:43520\n"
    "v_mfma_f32_16x16x32_bf16 v[48:51], a[208:211], v[104:107], v[48:51]\n"
    "s_add_u32 m0, s53, 114688\n"
    "ds_read_b64_tr_b16 a[220:221], v147 offset:43008\n"
    "v_mfma_f32_16x16x32_bf16 v[64:67], a[208:211], v[108:111], v[64:67]\n"
    "s_waitcnt vmcnt(6) lgkmcnt(3)\n"
    "buffer_load_dwordx4 v182, s[4:7], s28 offen lds\n"
    "v_mfma_f32_16x16x32_bf16 v[20:23], a[212:215], v[96:99], v[20:23]\n"
    "s_barrier\n"
    "ds_read_b64_tr_b16 a[222:223], v147 offset:43520\n"
    "v_mfma_f32_16x16x32_bf16 v[36:39], a[212:215], v[100:103], v[36:39]\n"
    "ds_read_b64_tr_b16 a[224:225], v145 offset:0\n"
    "v_mfma_f32_16x16x32_bf16 v[52:55], a[212:215], v[104:107], v[52:55]\n"
    "s_add_u32 m0, s53, 118784\n"
    "ds_read_b64_tr_b16 a[226:227], v145 offset:512\n"
    "v_mfma_f32_16x16x32_bf16 v[68:71], a[212:215], v[108:111], v[68:71]\n"
    "s_waitcnt lgkmcnt(4)\n"
    "buffer_load_dwordx4 v182, s[4:7], s29 offen lds\n"
    "v_mfma_f32_16x16x32_bf16 v[24:27], a[216:219], v[96:99], v[24:27]\n"
    "ds_read_b128 v[80:83], v139 offset:16384\n"
    "v_mfma_f32_16x16x32_bf16 v[40:43], a[216:219], v[100:103], v[40:43]\n"
    "ds_read_b128 v[84:87], v139 offset:20480\n"
    "v_mfma_f32_16x16x32_bf16 v[56:59], a[216:219], v[104:107], v[56:59]\n"
    "s_add_u32 m0, s53, 122880\n"
    "ds_read_b128 v[88:91], v139 offset:24576\n"
    "v_mfma_f32_16x16x32_bf16 v[72:75], a[216:219], v[108:111], v[72:75]\n"
    "s_waitcnt lgkmcnt(5)\n"
    "buffer_load_dwordx4 v182, s[4:7], s30 offen lds\n"
    "v_mfma_f32_16x16x32_bf16 v[28:31], a[220:223], v[96:99], v[28:31]\n"
    "ds_read_b128 v[92:95], v139 offset:28672\n"
    "v_mfma_f32_16x16x32_bf16 v[44:47], a[220:223], v[100:103], v[44:47]\n"
    "ds_read_b64_tr_b16 a[228:229], v148 offset:0\n"
    "v_mfma_f32_16x16x32_bf16 v[60:63], a[220:223], v[104:107], v[60:63]\n"
    "s_add_u32 m0, s53, 126976\n"
    "ds_read_b64_tr_b16 a[230:231], v148 offset:512\n"
    "v_mfma_f32_16x16x32_bf16 v[76:79], a[220:223], v[108:111], v[76:79]\n"
    "s_waitcnt lgkmcnt(3)\n"
    "buffer_load_dwordx4 v182, s[4:7], s31 offen lds\n"
    "v_mfma_f32_16x16x32_bf16 v[16:19], a[224:227], v[80:83], v[16:19]\n"
    "s_waitcnt lgkmcnt(2)\n"
    "ds_read_b64_tr_b16 a[232:233], v145 offset:2048\n"
    "v_mfma_f32_16x16x32_bf16 v[32:35], a[224:227], v[84:87], v[32:35]\n"
    "ds_read_b64_tr_b16 a[234:235], v145 offset:2560\n"
    "v_mfma_f32_16x16x32_bf16 v[48:51], a[224:227], v[88:91], v[48:51]\n"
    "s_add_u32 m0, s53, 131072\n"
    "ds_read_b64_tr_b16 a[236:237], v148 offset:2048\n"
    "v_mfma_f32_16x16x32_bf16 v[64:67], a[224:227], v[92:95], v[64:67]\n"
    "s_waitcnt vmcnt(8) lgkmcnt(3)\n"
    "buffer_load_dwordx4 v183, s[4:7], s24 offen lds\n"
    "v_mfma_f32_16x16x32_bf16 v[20:23], a[228:231], v[80:83], v[20:23]\n"
    "s_barrier\n"
    "ds_read_b64_tr_b16 a[238:239], v148 offset:2560\n"
    "v_mfma_f32_16x16x32_bf16 v[36:39], a[228:231], v[84:87], v[36:39]\n"
    "ds_read_b64_tr_b16 a[240:241], v145 offset:8192\n"
    "v_mfma_f32_16x16x32_bf16 v[52:55], a[228:231], v[88:91], v[52:55]\n"
    "s_add_u32 m0, s53, 135168\n"
    "ds_read_b64_tr_b16 a[242:243], v145 offset:8704\n"
    "v_mfma_f32_16x16x32_bf16 v[68:71], a[228:231], v[92:95], v[68:71]\n"
    "s_waitcnt lgkmcnt(4)\n"
    "buffer_load_dwordx4 v183, s[4:7], s25 offen lds\n"
    "v_mfma_f32_16x16x32_bf16 v[24:27], a[232:235], v[80:83], v[24:27]\n"
    "ds_read_b128 v[96:99], v142 offset:16384\n"
    "v_mfma_f32_16x16x32_bf16 v[40:43], a[232:235], v[84:87], v[40:43]\n"
    "ds_read_b128 v[100:103], v142 offset:20480\n"
    "v_mfma_f32_16x16x32_bf16 v[56:59], a[232:235], v[88:91], v[56:59]\n"
    "s_add_u32 m0, s53, 139264\n"
    "ds_read_b128 v[104:107], v142 offset:24576\n"
    "v_mfma_f32_16x16x32_bf16 v[72:75], a[232:235], v[92:95], v[72:75]\n"
    "s_waitcnt lgkmcnt(5)\n"
    "buffer_load_dwordx4 v183, s[4:7], s26 offen lds\n"
    "v_mfma_f32_16x16x32_bf16 v[28:31], a[236:239], v[80:83], v[28:31]\n"
    "ds_read_b128 v[108:111], v142 offset:28672\n"
    "v_mfma_f32_16x16x32_bf16 v[44:47], a[236:239], v[84:87], v[44:47]\n"
    "ds_read_b64_tr_b16 a[244:245], v148 offset:8192\n"
    "v_mfma_f32_16x16x32_bf16 v[60:63], a[236:239], v[88:91], v[60:63]\n"
    "s_add_u32 m0, s53, 143360\n"
    "ds_read_b64_tr_b16 a[246:247], v148 offset:8704\n"
    "v_mfma_f32_16x16x32_bf16 v[76:79], a[236:239], v[92:95], v[76:79]\n"
    "s_waitcnt lgkmcnt(3)\n"
    "buffer_load_dwordx4 v183, s[4:7], s27 offen lds\n"
    "v_mfma_f32_16x16x32_bf16 v[16:19], a[240:243], v[96:99], v[16:19]\n"
    "s_waitcnt lgkmcnt(2)\n"
    "ds_read_b64_tr_b16 a[248:249], v145 offset:10240\n"
    "v_mfma_f32_16x16x32_bf16 v[32:35], a[240:243], v[100:103], v[32:35]\n"
    "ds_read_b64_tr_b16 a[250:251], v145 offset:10752\n"
    "v_mfma_f32_16x16x32_bf16 v[48:51], a[240:243], v[104:107], v[48:51]\n"
    "s_add_u32 m0, s53, 147456\n"
    "ds_read_b64_tr_b16 a[252:253], v148 offset:10240\n"
    "v_mfma_f32_16x16x32_bf16 v[64:67], a[240:243], v[108:111], v[64:67]\n"
    "s_waitcnt lgkmcnt(3)\n"
    "buffer_load_dwordx4 v183, s[4:7], s28 offen lds\n"
    "v_mfma_f32_16x16x32_bf16 v[20:23], a[244:247], v[96:99], v[20:23]\n"
    "ds_read_b64_tr_b16 a[254:255], v148 offset:10752\n"
    "v_mfma_f32_16x16x32_bf16 v[36:39], a[244:247], v[100:103], v[36:39]\n"
    "v_mfma_f32_16x16x32_bf16 v[52:55], a[244:247], v[104:107], v[52:55]\n"
    "s_add_u32 m0, s53, 151552\n"
    "v_mfma_f32_16x16x32_bf16 v[68:71], a[244:247], v[108:111], v[68:71]\n"
    "s_waitcnt lgkmcnt(2)\n"
    "buffer_load_dwordx4 v183, s[4:7], s29 offen lds\n"
    "v_mfma_f32_16x16x32_bf16 v[24:27], a[248:251], v[96:99], v[24:27]\n"
    "v_mfma_f32_16x16x32_bf16 v[40:43], a[248:251], v[100:103], v[40:43]\n"
    "v_mfma_f32_16x16x32_bf16 v[56:59], a[248:251], v[104:107], v[56:59]\n"
    "s_add_u32 m0, s53, 155648\n"
    "v_mfma_f32_16x16x32_bf16 v[72:75], a[248:251], v[108:111], v[72:75]\n"
    "s_waitcnt lgkmcnt(0)\n"
    "buffer_load_dwordx4 v183, s[4:7], s30 offen lds\n"
    "v_mfma_f32_16x16x32_bf16 v[28:31], a[252:255], v[96:99], v[28:31]\n"
    "v_mfma_f32_16x16x32_bf16 v[44:47], a[252:255], v[100:103], v[44:47]\n"
    "v_mfma_f32_16x16x32_bf16 v[60:63], a[252:255], v[104:107], v[60:63]\n"
    "s_add_u32 m0, s53, 159744\n"
    "v_mfma_f32_16x16x32_bf16 v[76:79], a[252:255], v[108:111], v[76:79]\n"
    "buffer_load_dwordx4 v183, s[4:7], s31 offen lds\n"
    "s_nop 4\n"
    "v_pk_mul_f32 v[112:113], v[16:17], v[16:17]\n"
    "v_pk_mul_f32 v[114:115], v[18:19], v[18:19]\n"
    "v_pk_mul_f32 v[116:117], v[20:21], v[20:21]\n"
    "v_pk_mul_f32 v[118:119], v[22:23], v[22:23]\n"
    "v_pk_fma_f32 v[112:113], v[112:113], v[130:131], v[128:129]\n"
    "v_pk_fma_f32 v[114:115], v[114:115], v[130:131], v[128:129]\n"
    "v_pk_fma_f32 v[116:117], v[116:117], v[130:131], v[128:129]\n"
    "v_pk_fma_f32 v[118:119], v[118:119], v[130:131], v[128:129]\n"
    "v_pk_mul_f32 v[112:113], v[112:113], v[16:17]\n"
    "v_pk_mul_f32 v[114:115], v[114:115], v[18:19]\n"
    "v_pk_mul_f32 v[116:117], v[116:117], v[20:21]\n"
    "v_pk_mul_f32 v[118:119], v[118:119], v[22:23]\n"
    "v_exp_f32 v112, v112\n"
    "v_exp_f32 v113, v113\n"
    "v_exp_f32 v114, v114\n"
    "v_exp_f32 v115, v115\n"
    "v_exp_f32 v116, v116\n"
    "v_exp_f32 v117, v117\n"
    "v_exp_f32 v118, v118\n"
    "v_exp_f32 v119, v119\n"
    "v_pk_add_f32 v[112:113], v[112:113], 1.0 op_sel_hi:[1,0]\n"
    "v_pk_add_f32 v[114:115], v[114:115], 1.0 op_sel_hi:[1,0]\n"
    "v_pk_add_f32 v[116:117], v[116:117], 1.0 op_sel_hi:[1,0]\n"
    "v_pk_add_f32 v[118:119], v[118:119], 1.0 op_sel_hi:[1,0]\n"
    "v_rcp_f32 v112, v112\n"
    "v_rcp_f32 v113, v113\n"
    "v_rcp_f32 v114, v114\n"
    "v_rcp_f32 v115, v115\n"
    "v_rcp_f32 v116, v116\n"
    "v_rcp_f32 v117, v117\n"
    "s_add_u32 m0, s53, 0\n"
    "s_nop 0\n"
    "buffer_load_dwordx4 v184, s[4:7], s24 offen lds\n"
    "v_rcp_f32 v118, v118\n"
    "v_rcp_f32 v119, v119\n"
    "v_pk_mul_f32 v[112:113], v[112:113], v[16:17]\n"
    "v_pk_mul_f32 v[114:115], v[114:115], v[18:19]\n"
    "v_pk_mul_f32 v[116:117], v[116:117], v[20:21]\n"
    "v_pk_mul_f32 v[118:119], v[118:119], v[22:23]\n"
    "v_pk_mul_f32 v[112:113], v[112:113], v[24:25]\n"
    "v_pk_mul_f32 v[114:115], v[114:115], v[26:27]\n"
    "v_pk_mul_f32 v[116:117], v[116:117], v[28:29]\n"
    "v_pk_mul_f32 v[118:119], v[118:119], v[30:31]\n"
    "v_cvt_pk_bf16_f32 v120, v112, v113\n"
    "v_cvt_pk_bf16_f32 v121, v114, v115\n"
    "v_cvt_pk_bf16_f32 v122, v116, v117\n"
    "v_cvt_pk_bf16_f32 v123, v118, v119\n"
    "buffer_store_dwordx4 v[120:123], v186, s[20:23], s44 offen sc1\n"
    "buffer_store_dword v133, v156, s[20:23], s35 offen sc1\n"
    "buffer_store_dwordx2 v[4:5], v6, s[20:23], s91 offen sc1\n"
    "v_pk_mul_f32 v[112:113], v[32:33], v[32:33]\n"
    "v_pk_mul_f32 v[114:115], v[34:35], v[34:35]\n"
    "v_pk_mul_f32 v[116:117], v[36:37], v[36:37]\n"
    "v_pk_mul_f32 v[118:119], v[38:39], v[38:39]\n"
    "v_pk_fma_f32 v[112:113], v[112:113], v[130:131], v[128:129]\n"
    "v_pk_fma_f32 v[114:115], v[114:115], v[130:131], v[128:129]\n"
    "v_pk_fma_f32 v[116:117], v[116:117], v[130:131], v[128:129]\n"
    "v_pk_fma_f32 v[118:119], v[118:119], v[130:131], v[128:129]\n"
    "v_pk_mul_f32 v[112:113], v[112:113], v[32:33]\n"
    "v_pk_mul_f32 v[114:115], v[114:115], v[34:35]\n"
    "v_pk_mul_f32 v[116:117], v[116:117], v[36:37]\n"
    "v_pk_mul_f32 v[118:119], v[118:119], v[38:39]\n"
    "v_exp_f32 v112, v112\n"
    "v_exp_f32 v113, v113\n"
    "v_exp_f32 v114, v114\n"
    "s_add_u32 m0, s53, 4096\n"
    "s_nop 0\n"
    "buffer_load_dwordx4 v184, s[4:7], s25 offen lds\n"
    "v_exp_f32 v115, v115\n"
    "v_exp_f32 v116, v116\n"
    "v_exp_f32 v117, v117\n"
    "v_exp_f32 v118, v118\n"
    "v_exp_f32 v119, v119\n"
    "v_pk_add_f32 v[112:113], v[112:113], 1.0 op_sel_hi:[1,0]\n"
    "v_pk_add_f32 v[114:115], v[114:115], 1.0 op_sel_hi:[1,0]\n"
    "v_pk_add_f32 v[116:117], v[116:117], 1.0 op_sel_hi:[1,0]\n"
    "v_pk_add_f32 v[118:119], v[118:119], 1.0 op_sel_hi:[1,0]\n"
    "v_rcp_f32 v112, v112\n"
    "v_rcp_f32 v113, v113\n"
    "v_rcp_f32 v114, v114\n"
    "s_add_u32 m0, s53, 8192\n"
    "s_nop 0\n"
    "buffer_load_dwordx4 v184, s[4:7], s26 offen lds\n"
    "v_rcp_f32 v115, v115\n"
    "v_rcp_f32 v116, v116\n"
    "v_rcp_f32 v117, v117\n"
    "v_rcp_f32 v118, v118\n"
    "v_rcp_f32 v119, v119\n"
    "v_pk_mul_f32 v[112:113], v[112:113], v[32:33]\n"
    "v_pk_mul_f32 v[114:115], v[114:115], v[34:35]\n"
    "v_pk_mul_f32 v[116:117], v[116:117], v[36:37]\n"
    "v_pk_mul_f32 v[118:119], v[118:119], v[38:39]\n"
    "v_pk_mul_f32 v[112:113], v[112:113], v[40:41]\n"
    "v_pk_mul_f32 v[114:115], v[114:115], v[42:43]\n"
    "v_pk_mul_f32 v[116:117], v[116:117], v[44:45]\n"
    "v_pk_mul_f32 v[118:119], v[118:119], v[46:47]\n"
    "v_cvt_pk_bf16_f32 v124, v112, v113\n"
    "v_cvt_pk_bf16_f32 v125, v114, v115\n"
    "s_add_u32 m0, s53, 12288\n"
    "s_nop 0\n"
    "buffer_load_dwordx4 v184, s[4:7], s27 offen lds\n"
    "v_cvt_pk_bf16_f32 v126, v116, v117\n"
    "v_cvt_pk_bf16_f32 v127, v118, v119\n"
    "buffer_store_dwordx4 v[124:127], v187, s[20:23], s44 offen sc1\n"
    "v_pk_mul_f32 v[112:113], v[48:49], v[48:49]\n"
    "v_pk_mul_f32 v[114:115], v[50:51], v[50:51]\n"
    "v_pk_mul_f32 v[116:117], v[52:53], v[52:53]\n"
    "v_pk_mul_f32 v[118:119], v[54:55], v[54:55]\n"
    "v_pk_fma_f32 v[112:113], v[112:113], v[130:131], v[128:129]\n"
    "v_pk_fma_f32 v[114:115], v[114:115], v[130:131], v[128:129]\n"
    "v_pk_fma_f32 v[116:117], v[116:117], v[130:131], v[128:129]\n"
    "v_pk_fma_f32 v[118:119], v[118:119], v[130:131], v[128:129]\n"
    "v_pk_mul_f32 v[112:113], v[112:113], v[48:49]\n"
    "v_pk_mul_f32 v[114:115], v[114:115], v[50:51]\n"
    "v_pk_mul_f32 v[116:117], v[116:117], v[52:53]\n"
    "v_pk_mul_f32 v[118:119], v[118:119], v[54:55]\n"
    "v_exp_f32 v112, v112\n"
    "v_exp_f32 v113, v113\n"
    "v_exp_f32 v114, v114\n"
    "s_add_u32 m0, s53, 16384\n"
    "s_nop 0\n"
    "buffer_load_dwordx4 v184, s[4:7], s28 offen lds\n"
    "s_waitcnt vmcnt(17)\n"
    "s_barrier\n"
    "ds_read_b128 v[80:83], v139 offset:32768\n"
    "ds_read_b128 v[84:87], v142 offset:32768\n"
    "v_exp_f32 v115, v115\n"
    "L_stub1:\n"
    "s_cbranch_execz L_warm_back\n"
    "v_exp_f32 v116, v116\n"
    "v_exp_f32 v117, v117\n"
    "v_exp_f32 v118, v118\n"
    "v_exp_f32 v119, v119\n"
    "v_pk_add_f32 v[112:113], v[112:113], 1.0 op_sel_hi:[1,0]\n"
    "v_pk_add_f32 v[114:115], v[114:115], 1.0 op_sel_hi:[1,0]\n"
    "v_pk_add_f32 v[116:117], v[116:117], 1.0 op_sel_hi:[1,0]\n"
    "v_pk_add_f32 v[118:119], v[118:119], 1.0 op_sel_hi:[1,0]\n"
    "v_rcp_f32 v112, v112\n"
    "v_rcp_f32 v113, v113\n"
    "v_rcp_f32 v114, v114\n"
    "s_add_u32 m0, s53, 20480\n"
    "s_nop 0\n"
    "buffer_load_dwordx4 v184, s[4:7], s29 offen lds\n"
    "v_rcp_f32 v115, v115\n"
    "v_rcp_f32 v116, v116\n"
    "v_rcp_f32 v117, v117\n"
    "v_rcp_f32 v118, v118\n"
    "v_rcp_f32 v119, v119\n"
    "v_pk_mul_f32 v[112:113], v[112:113], v[48:49]\n"
    "v_pk_mul_f32 v[114:115], v[114:115], v[50:51]\n"
    "v_pk_mul_f32 v[116:117], v[116:117], v[52:53]\n"
    "v_pk_mul_f32 v[118:119], v[118:119], v[54:55]\n"
    "v_pk_mul_f32 v[112:113], v[112:113], v[56:57]\n"
    "v_pk_mul_f32 v[114:115], v[114:115], v[58:59]\n"
    "v_pk_mul_f32 v[116:117], v[116:117], v[60:61]\n"
    "v_pk_mul_f32 v[118:119], v[118:119], v[62:63]\n"
    "v_cvt_pk_bf16_f32 v120, v112, v113\n"
    "v_cvt_pk_bf16_f32 v121, v114, v115\n"
    "s_add_u32 m0, s53, 24576\n"
    "s_nop 0\n"
    "buffer_load_dwordx4 v184, s[4:7], s30 offen lds\n"
    "v_cvt_pk_bf16_f32 v122, v116, v117\n"
    "v_cvt_pk_bf16_f32 v123, v118, v119\n"
    "buffer_store_dwordx4 v[120:123], v186, s[20:23], s45 offen sc1\n"
    "s_waitcnt lgkmcnt(0)\n"
    "v_mfma_f32_16x16x32_bf16 v[16:19], a[0:3], v[80:83], 0\n"
    "s_add_u32 m0, s53, 28672\n"
    "ds_read_b128 v[88:91], v139 offset:36864\n"
    "v_mul_f32 v112, v64, v64\n"
    "v_mfma_f32_16x16x32_bf16 v[16:19], a[16:19], v[84:87], v[16:19]\n"
    "v_mul_f32 v113, v65, v65\n"
    "v_mul_f32 v114, v66, v66\n"
    "v_mfma_f32_16x16x32_bf16 v[20:23], a[4:7], v[80:83], 0\n"
    "ds_read_b128 v[92:95], v142 offset:36864\n"
    "v_mul_f32 v115, v67, v67\n"
    "v_mfma_f32_16x16x32_bf16 v[20:23], a[20:23], v[84:87], v[20:23]\n"
    "buffer_load_dwordx4 v184, s[4:7], s31 offen lds\n"
    "v_mfma_f32_16x16x32_bf16 v[24:27], a[8:11], v[80:83], 0\n"
    "s_add_u32 m0, s64, 32768\n"
    "v_fma_f32 v112, v112, v130, v128\n"
    "v_fma_f32 v113, v113, v130, v128\n"
    "v_mfma_f32_16x16x32_bf16 v[24:27], a[24:27], v[84:87], v[24:27]\n"
    "v_fma_f32 v114, v114, v130, v128\n"
    "v_fma_f32 v115, v115, v130, v128\n"
    "v_mfma_f32_16x16x32_bf16 v[28:31], a[12:15], v[80:83], 0\n"
    "v_mul_f32 v112, v112, v64\n"
    "v_mul_f32 v113, v113, v65\n"
    "v_mfma_f32_16x16x32_bf16 v[28:31], a[28:31], v[84:87], v[28:31]\n"
    "s_waitcnt lgkmcnt(0)\n"
    "v_mul_f32 v114, v114, v66\n"
    "v_mul_f32 v115, v115, v67\n"
    "v_mfma_f32_16x16x32_bf16 v[16:19], a[32:35], v[88:91], v[16:19]\n"
    "ds_read_b128 v[96:99], v139 offset:40960\n"
    "v_mfma_f32_16x16x32_bf16 v[16:19], a[48:51], v[92:95], v[16:19]\n"
    "buffer_load_dwordx4 v151, s[12:15], s84 offen lds\n"
    "v_mfma_f32_16x16x32_bf16 v[20:23], a[36:39], v[88:91], v[20:23]\n"
    "ds_read_b128 v[100:103], v142 offset:40960\n"
    "v_mfma_f32_16x16x32_bf16 v[20:23], a[52:55], v[92:95], v[20:23]\n"
    "v_exp_f32 v112, v112\n"
    "v_mfma_f32_16x16x32_bf16 v[24:27], a[40:43], v[88:91], v[24:27]\n"
    "v_exp_f32 v113, v113\n"
    "v_mfma_f32_16x16x32_bf16 v[24:27], a[56:59], v[92:95], v[24:27]\n"
    "buffer_load_dwordx4 v152, s[12:15], s84 offen offset:1024 lds\n"
    "v_mfma_f32_16x16x32_bf16 v[28:31], a[44:47], v[88:91], v[28:31]\n"
    "v_exp_f32 v114, v114\n"
    "v_mfma_f32_16x16x32_bf16 v[28:31], a[60:63], v[92:95], v[28:31]\n"
    "s_waitcnt lgkmcnt(0)\n"
    "v_exp_f32 v115, v115\n"
    "v_mfma_f32_16x16x32_bf16 v[16:19], a[64:67], v[96:99], v[16:19]\n"
    "ds_read_b128 v[104:107], v139 offset:45056\n"
    "v_add_f32 v112, 1.0, v112\n"
    "v_mfma_f32_16x16x32_bf16 v[16:19], a[80:83], v[100:103], v[16:19]\n"
    "buffer_load_dwordx4 v153, s[12:15], s84 offen offset:2048 lds\n"
    "v_mfma_f32_16x16x32_bf16 v[20:23], a[68:71], v[96:99], v[20:23]\n"
    "ds_read_b128 v[108:111], v142 offset:45056\n"
    "v_add_f32 v113, 1.0, v113\n"
    "v_mfma_f32_16x16x32_bf16 v[20:23], a[84:87], v[100:103], v[20:23]\n"
    "v_add_f32 v114, 1.0, v114\n"
    "v_add_f32 v115, 1.0, v115\n"
    "v_mfma_f32_16x16x32_bf16 v[24:27], a[72:75], v[96:99], v[24:27]\n"
    "v_rcp_f32 v112, v112\n"
    "v_mfma_f32_16x16x32_bf16 v[24:27], a[88:91], v[100:103], v[24:27]\n"
    "buffer_load_dwordx4 v154, s[12:15], s84 offen offset:3072 lds\n"
    "v_mfma_f32_16x16x32_bf16 v[28:31], a[76:79], v[96:99], v[28:31]\n"
    "s_add_u32 m0, s64, 49152\n"
    "v_rcp_f32 v113, v113\n"
    "v_mfma_f32_16x16x32_bf16 v[28:31], a[92:95], v[100:103], v[28:31]\n"
    "s_waitcnt lgkmcnt(0)\n"
    "v_rcp_f32 v114, v114\n"
    "v_mfma_f32_16x16x32_bf16 v[16:19], a[96:99], v[104:107], v[16:19]\n"
    "ds_read_b128 v[80:83], v139 offset:49152\n"
    "v_mfma_f32_16x16x32_bf16 v[16:19], a[112:115], v[108:111], v[16:19]\n"
    "buffer_load_dwordx4 v151, s[12:15], s85 offen lds\n"
    "v_mfma_f32_16x16x32_bf16 v[20:23], a[100:103], v[104:107], v[20:23]\n"
    "ds_read_b128 v[84:87], v142 offset:49152\n"
    "v_rcp_f32 v115, v115\n"
    "v_mfma_f32_16x16x32_bf16 v[20:23], a[116:119], v[108:111], v[20:23]\n"
    "v_mul_f32 v112, v112, v64\n"
    "v_mul_f32 v113, v113, v65\n"
    "v_mul_f32 v114, v114, v66\n"
    "v_mfma_f32_16x16x32_bf16 v[24:27], a[104:107], v[104:107], v[24:27]\n"
    "s_waitcnt vmcnt(10)\n"
    "buffer_store_dword v133, v137, s[20:23], s48 offen sc1\n"
    "v_mfma_f32_16x16x32_bf16 v[24:27], a[120:123], v[108:111], v[24:27]\n"
    "v_mul_f32 v115, v115, v67\n"
    "v_mul_f32 v112, v112, v72\n"
    "v_mfma_f32_16x16x32_bf16 v[28:31], a[108:111], v[104:107], v[28:31]\n"
    "v_mul_f32 v113, v113, v73\n"
    "v_mul_f32 v114, v114, v74\n"
    "v_mfma_f32_16x16x32_bf16 v[28:31], a[124:127], v[108:111], v[28:31]\n"
    "s_waitcnt lgkmcnt(0)\n"
    "v_mul_f32 v115, v115, v75\n"
    "v_cvt_pk_bf16_f32 v124, v112, v113\n"
    "v_mfma_f32_16x16x32_bf16 v[16:19], a[128:131], v[80:83], v[16:19]\n"
    "ds_read_b128 v[88:91], v139 offset:53248\n"
    "v_cvt_pk_bf16_f32 v125, v114, v115\n"
    "v_mfma_f32_16x16x32_bf16 v[16:19], a[144:147], v[84:87], v[16:19]\n"
    "buffer_load_dwordx4 v152, s[12:15], s85 offen offset:1024 lds\n"
    "v_mfma_f32_16x16x32_bf16 v[20:23], a[132:135], v[80:83], v[20:23]\n"
    "ds_read_b128 v[92:95], v142 offset:53248\n"
    "v_mul_f32 v116, v68, v68\n"
    "v_mfma_f32_16x16x32_bf16 v[20:23], a[148:151], v[84:87], v[20:23]\n"
    "v_mul_f32 v117, v69, v69\n"
    "v_mul_f32 v118, v70, v70\n"
    "v_mul_f32 v119, v71, v71\n"
    "v_mfma_f32_16x16x32_bf16 v[24:27], a[136:139], v[80:83], v[24:27]\n"
    "v_fma_f32 v116, v116, v130, v128\n"
    "v_fma_f32 v117, v117, v130, v128\n"
    "v_mfma_f32_16x16x32_bf16 v[24:27], a[152:155], v[84:87], v[24:27]\n"
    "buffer_load_dwordx4 v153, s[12:15], s85 offen offset:2048 lds\n"
    "v_mfma_f32_16x16x32_bf16 v[28:31], a[140:143], v[80:83], v[28:31]\n"
    "v_fma_f32 v118, v118, v130, v128\n"
    "v_fma_f32 v119, v119, v130, v128\n"
    "v_mfma_f32_16x16x32_bf16 v[28:31], a[156:159], v[84:87], v[28:31]\n"
    "s_waitcnt lgkmcnt(0)\n"
    "v_mul_f32 v116, v116, v68\n"
    "v_mul_f32 v117, v117, v69\n"
    "v_mfma_f32_16x16x32_bf16 v[16:19], a[160:163], v[88:91], v[16:19]\n"
    "ds_read_b128 v[96:99], v139 offset:57344\n"
    "v_mul_f32 v118, v118, v70\n"
    "v_mfma_f32_16x16x32_bf16 v[16:19], a[176:179], v[92:95], v[16:19]\n"
    "v_mul_f32 v119, v119, v71\n"
    "v_mfma_f32_16x16x32_bf16 v[20:23], a[164:167], v[88:91], v[20:23]\n"
    "ds_read_b128 v[100:103], v142 offset:57344\n"
    "v_mfma_f32_16x16x32_bf16 v[20:23], a[180:183], v[92:95], v[20:23]\n"
    "v_exp_f32 v116, v116\n"
    "v_mfma_f32_16x16x32_bf16 v[24:27], a[168:171], v[88:91], v[24:27]\n"
    "v_exp_f32 v117, v117\n"
    "v_mfma_f32_16x16x32_bf16 v[24:27], a[184:187], v[92:95], v[24:27]\n"
    "v_exp_f32 v118, v118\n"
    "v_mfma_f32_16x16x32_bf16 v[28:31], a[172:175], v[88:91], v[28:31]\n"
    "v_exp_f32 v119, v119\n"
    "v_mfma_f32_16x16x32_bf16 v[28:31], a[188:191], v[92:95], v[28:31]\n"
    "s_waitcnt lgkmcnt(0)\n"
    "buffer_load_dwordx4 v154, s[12:15], s85 offen offset:3072 lds\n"
    "v_mfma_f32_16x16x32_bf16 v[16:19], a[192:195], v[96:99], v[16:19]\n"
    "s_add_u32 m0, s64, 65536\n"
    "ds_read_b128 v[104:107], v139 offset:61440\n"
    "v_add_f32 v116, 1.0, v116\n"
    "v_mfma_f32_16x16x32_bf16 v[16:19], a[208:211], v[100:103], v[16:19]\n"
    "v_add_f32 v117, 1.0, v117\n"
    "v_add_f32 v118, 1.0, v118\n"
    "v_mfma_f32_16x16x32_bf16 v[20:23], a[196:199], v[96:99], v[20:23]\n"
    "ds_read_b128 v[108:111], v142 offset:61440\n"
    "v_add_f32 v119, 1.0, v119\n"
    "v_mfma_f32_16x16x32_bf16 v[20:23], a[212:215], v[100:103], v[20:23]\n"
    "buffer_load_dwordx4 v151, s[12:15], s86 offen lds\n"
    "v_mfma_f32_16x16x32_bf16 v[24:27], a[200:203], v[96:99], v[24:27]\n"
    "v_rcp_f32 v116, v116\n"
    "v_mfma_f32_16x16x32_bf16 v[24:27], a[216:219], v[100:103], v[24:27]\n"
    "v_rcp_f32 v117, v117\n"
    "v_mfma_f32_16x16x32_bf16 v[28:31], a[204:207], v[96:99], v[28:31]\n"
    "v_rcp_f32 v118, v118\n"
    "v_mfma_f32_16x16x32_bf16 v[28:31], a[220:223], v[100:103], v[28:31]\n"
    "s_waitcnt lgkmcnt(0)\n"
    "buffer_load_dwordx4 v152, s[12:15], s86 offen offset:1024 lds\n"
    "v_mfma_f32_16x16x32_bf16 v[16:19], a[224:227], v[104:107], v[16:19]\n"
    "s_barrier\n"
    "ds_read_b128 v[80:83], v140 offset:0\n"
    "v_rcp_f32 v119, v119\n"
    "v_mfma_f32_16x16x32_bf16 v[16:19], a[240:243], v[108:111], v[16:19]\n"
    "v_mul_f32 v116, v116, v68\n"
    "v_mul_f32 v117, v117, v69\n"
    "v_mul_f32 v118, v118, v70\n"
    "v_mfma_f32_16x16x32_bf16 v[20:23], a[228:231], v[104:107], v[20:23]\n"
    "ds_read_b128 v[84:87], v143 offset:0\n"
    "v_mul_f32 v119, v119, v71\n"
    "v_mfma_f32_16x16x32_bf16 v[20:23], a[244:247], v[108:111], v[20:23]\n"
    "buffer_load_dwordx4 v153, s[12:15], s86 offen offset:2048 lds\n"
    "v_mfma_f32_16x16x32_bf16 v[24:27], a[232:235], v[104:107], v[24:27]\n"
    "v_mul_f32 v116, v116, v76\n"
    "v_mul_f32 v117, v117, v77\n"
    "v_mul_f32 v118, v118, v78\n"
    "v_mfma_f32_16x16x32_bf16 v[24:27], a[248:251], v[108:111], v[24:27]\n"
    "v_mul_f32 v119, v119, v79\n"
    "v_cvt_pk_bf16_f32 v126, v116, v117\n"
    "v_mfma_f32_16x16x32_bf16 v[28:31], a[236:239], v[104:107], v[28:31]\n"
    "v_cvt_pk_bf16_f32 v127, v118, v119\n"
    "v_mfma_f32_16x16x32_bf16 v[28:31], a[252:255], v[108:111], v[28:31]\n"
    "s_waitcnt lgkmcnt(0)\n"
    "buffer_store_dwordx4 v[124:127], v187, s[20:23], s45 offen sc1\n"
    "v_mfma_f32_16x16x32_bf16 v[32:35], a[0:3], v[80:83], 0\n"
    "ds_read_b128 v[88:91], v140 offset:4096\n"
    "v_mfma_f32_16x16x32_bf16 v[32:35], a[16:19], v[84:87], v[32:35]\n"
    "v_mfma_f32_16x16x32_bf16 v[36:39], a[4:7], v[80:83], 0\n"
    "ds_read_b128 v[92:95], v143 offset:4096\n"
    "v_mfma_f32_16x16x32_bf16 v[36:39], a[20:23], v[84:87], v[36:39]\n"
    "buffer_load_dwordx4 v154, s[12:15], s86 offen offset:3072 lds\n"
    "v_mfma_f32_16x16x32_bf16 v[40:43], a[8:11], v[80:83], 0\n"
    "s_add_u32 m0, s64, 81920\n"
    "v_mfma_f32_16x16x32_bf16 v[40:43], a[24:27], v[84:87], v[40:43]\n"
    "v_mul_f32 v112, v16, v16\n"
    "v_mul_f32 v113, v17, v17\n"
    "v_mul_f32 v114, v18, v18\n"
    "v_mfma_f32_16x16x32_bf16 v[44:47], a[12:15], v[80:83], 0\n"
    "v_mul_f32 v115, v19, v19\n"
    "v_fma_f32 v112, v112, v130, v128\n"
    "v_mfma_f32_16x16x32_bf16 v[44:47], a[28:31], v[84:87], v[44:47]\n"
    "s_waitcnt lgkmcnt(0)\n"
    "buffer_load_dwordx4 v151, s[12:15], s87 offen lds\n"
    "v_mfma_f32_16x16x32_bf16 v[32:35], a[32:35], v[88:91], v[32:35]\n"
    "ds_read_b128 v[96:99], v140 offset:8192\n"
    "v_fma_f32 v113, v113, v130, v128\n"
    "v_fma_f32 v114, v114, v130, v128\n"
    "v_mfma_f32_16x16x32_bf16 v[32:35], a[48:51], v[92:95], v[32:35]\n"
    "v_fma_f32 v115, v115, v130, v128\n"
    "v_mul_f32 v112, v112, v16\n"
    "v_mfma_f32_16x16x32_bf16 v[36:39], a[36:39], v[88:91], v[36:39]\n"
    "ds_read_b128 v[100:103], v143 offset:8192\n"
    "v_mul_f32 v113, v113, v17\n"
    "v_mfma_f32_16x16x32_bf16 v[36:39], a[52:55], v[92:95], v[36:39]\n"
    "buffer_load_dwordx4 v152, s[12:15], s87 offen offset:1024 lds\n"
    "v_mfma_f32_16x16x32_bf16 v[40:43], a[40:43], v[88:91], v[40:43]\n"
    "v_mul_f32 v114, v114, v18\n"
    "v_mul_f32 v115, v115, v19\n"
    "v_mfma_f32_16x16x32_bf16 v[40:43], a[56:59], v[92:95], v[40:43]\n"
    "v_exp_f32 v112, v112\n"
    "v_mfma_f32_16x16x32_bf16 v[44:47], a[44:47], v[88:91], v[44:47]\n"
    "v_exp_f32 v113, v113\n"
    "v_mfma_f32_16x16x32_bf16 v[44:47], a[60:63], v[92:95], v[44:47]\n"
    "s_waitcnt lgkmcnt(0)\n"
    "buffer_load_dwordx4 v153, s[12:15], s87 offen offset:2048 lds\n"
    "v_mfma_f32_16x16x32_bf16 v[32:35], a[64:67], v[96:99], v[32:35]\n"
    "ds_read_b128 v[104:107], v140 offset:12288\n"
    "v_exp_f32 v114, v114\n"
    "v_mfma_f32_16x16x32_bf16 v[32:35], a[80:83], v[100:103], v[32:35]\n"
    "v_exp_f32 v115, v115\n"
    "v_add_f32 v112, 1.0, v112\n"
    "v_mfma_f32_16x16x32_bf16 v[36:39], a[68:71], v[96:99], v[36:39]\n"
    "ds_read_b128 v[108:111], v143 offset:12288\n"
    "v_add_f32 v113, 1.0, v113\n"
    "v_mfma_f32_16x16x32_bf16 v[36:39], a[84:87], v[100:103], v[36:39]\n"
    "buffer_load_dwordx4 v154, s[12:15], s87 offen offset:3072 lds\n"
    "v_mfma_f32_16x16x32_bf16 v[40:43], a[72:75], v[96:99], v[40:43]\n"
    "s_add_u32 m0, s53, 98304\n"
    "v_add_f32 v114, 1.0, v114\n"
    "v_add_f32 v115, 1.0, v115\n"
    "v_mfma_f32_16x16x32_bf16 v[40:43], a[88:91], v[100:103], v[40:43]\n"
    "v_rcp_f32 v112, v112\n"
    "v_mfma_f32_16x16x32_bf16 v[44:47], a[76:79], v[96:99], v[44:47]\n"
    "v_rcp_f32 v113, v113\n"
    "v_mfma_f32_16x16x32_bf16 v[44:47], a[92:95], v[100:103], v[44:47]\n"
    "s_waitcnt lgkmcnt(0)\n"
    "buffer_load_dwordx4 v185, s[4:7], s24 offen lds\n"
    "v_mfma_f32_16x16x32_bf16 v[32:35], a[96:99], v[104:107], v[32:35]\n"
    "s_add_u32 m0, s53, 102400\n"
    "ds_read_b128 v[80:83], v140 offset:16384\n"
    "v_mfma_f32_16x16x32_bf16 v[32:35], a[112:115], v[108:111], v[32:35]\n"
    "buffer_load_dwordx2 v[134:135], v157, s[20:23], 0 offen sc1\n"
    "v_mfma_f32_16x16x32_bf16 v[36:39], a[100:103], v[104:107], v[36:39]\n"
    "ds_read_b128 v[84:87], v143 offset:16384\n"
    "v_rcp_f32 v114, v114\n"
    "v_mfma_f32_16x16x32_bf16 v[36:39], a[116:119], v[108:111], v[36:39]\n"
    "v_rcp_f32 v115, v115\n"
    "v_mul_f32 v112, v112, v16\n"
    "v_mfma_f32_16x16x32_bf16 v[40:43], a[104:107], v[104:107], v[40:43]\n"
    "v_mul_f32 v113, v113, v17\n"
    "v_mul_f32 v114, v114, v18\n"
    "v_mul_f32 v115, v115, v19\n"
    "v_mfma_f32_16x16x32_bf16 v[40:43], a[120:123], v[108:111], v[40:43]\n"
    "buffer_load_dwordx4 v185, s[4:7], s25 offen lds\n"
    "v_mfma_f32_16x16x32_bf16 v[44:47], a[108:111], v[104:107], v[44:47]\n"
    "s_add_u32 m0, s53, 106496\n"
    "v_mul_f32 v112, v112, v24\n"
    "v_mul_f32 v113, v113, v25\n"
    "v_mul_f32 v114, v114, v26\n"
    "v_mfma_f32_16x16x32_bf16 v[44:47], a[124:127], v[108:111], v[44:47]\n"
    "s_waitcnt lgkmcnt(0)\n"
    "v_mul_f32 v115, v115, v27\n"
    "v_cvt_pk_bf16_f32 v120, v112, v113\n"
    "v_mfma_f32_16x16x32_bf16 v[32:35], a[128:131], v[80:83], v[32:35]\n"
    "ds_read_b128 v[88:91], v140 offset:20480\n"
    "v_cvt_pk_bf16_f32 v121, v114, v115\n"
    "v_mfma_f32_16x16x32_bf16 v[32:35], a[144:147], v[84:87], v[32:35]\n"
    "v_mul_f32 v116, v20, v20\n"
    "v_mul_f32 v117, v21, v21\n"
    "v_mfma_f32_16x16x32_bf16 v[36:39], a[132:135], v[80:83], v[36:39]\n"
    "ds_read_b128 v[92:95], v143 offset:20480\n"
    "v_mul_f32 v118, v22, v22\n"
    "v_mfma_f32_16x16x32_bf16 v[36:39], a[148:151], v[84:87], v[36:39]\n"
    "v_mul_f32 v119, v23, v23\n"
    "v_fma_f32 v116, v116, v130, v128\n"
    "v_mfma_f32_16x16x32_bf16 v[40:43], a[136:139], v[80:83], v[40:43]\n"
    "v_fma_f32 v117, v117, v130, v128\n"
    "v_fma_f32 v118, v118, v130, v128\n"
    "v_mfma_f32_16x16x32_bf16 v[40:43], a[152:155], v[84:87], v[40:43]\n"
    "v_fma_f32 v119, v119, v130, v128\n"
    "v_mul_f32 v116, v116, v20\n"
    "v_mfma_f32_16x16x32_bf16 v[44:47], a[140:143], v[80:83], v[44:47]\n"
    "v_mul_f32 v117, v117, v21\n"
    "v_mul_f32 v118, v118, v22\n"
    "v_mfma_f32_16x16x32_bf16 v[44:47], a[156:159], v[84:87], v[44:47]\n"
    "s_waitcnt lgkmcnt(0)\n"
    "v_mul_f32 v119, v119, v23\n"
    "v_mfma_f32_16x16x32_bf16 v[32:35], a[160:163], v[88:91], v[32:35]\n"
    "ds_read_b128 v[96:99], v140 offset:24576\n"
    "v_mfma_f32_16x16x32_bf16 v[32:35], a[176:179], v[92:95], v[32:35]\n"
    "v_exp_f32 v116, v116\n"
    "v_mfma_f32_16x16x32_bf16 v[36:39], a[164:167], v[88:91], v[36:39]\n"
    "ds_read_b128 v[100:103], v143 offset:24576\n"
    "v_mfma_f32_16x16x32_bf16 v[36:39], a[180:183], v[92:95], v[36:39]\n"
    "buffer_load_dwordx4 v185, s[4:7], s26 offen lds\n"
    "v_mfma_f32_16x16x32_bf16 v[40:43], a[168:171], v[88:91], v[40:43]\n"
    "s_add_u32 m0, s53, 110592\n"
    "v_exp_f32 v117, v117\n"
    "v_mfma_f32_16x16x32_bf16 v[40:43], a[184:187], v[92:95], v[40:43]\n"
    "v_exp_f32 v118, v118\n"
    "v_mfma_f32_16x16x32_bf16 v[44:47], a[172:175], v[88:91], v[44:47]\n"
    "v_exp_f32 v119, v119\n"
    "v_mfma_f32_16x16x32_bf16 v[44:47], a[188:191], v[92:95], v[44:47]\n"
    "s_waitcnt lgkmcnt(0)\n"
    "buffer_load_dwordx4 v185, s[4:7], s27 offen lds\n"
    "v_mfma_f32_16x16x32_bf16 v[32:35], a[192:195], v[96:99], v[32:35]\n"
    "s_add_u32 m0, s53, 114688\n"
    "ds_read_b128 v[104:107], v140 offset:28672\n"
    "v_add_f32 v116, 1.0, v116\n"
    "v_mfma_f32_16x16x32_bf16 v[32:35], a[208:211], v[100:103], v[32:35]\n"
    "s_waitcnt vmcnt(10)\n"
    "buffer_store_dword v133, v137, s[20:23], s49 offen sc1\n"
    "v_mfma_f32_16x16x32_bf16 v[36:39], a[196:199], v[96:99], v[36:39]\n"
    "ds_read_b128 v[108:111], v143 offset:28672\n"
    "v_add_f32 v117, 1.0, v117\n"
    "v_add_f32 v118, 1.0, v118\n"
    "v_mfma_f32_16x16x32_bf16 v[36:39], a[212:215], v[100:103], v[36:39]\n"
    "v_add_f32 v119, 1.0, v119\n"
    "v_rcp_f32 v116, v116\n"
    "v_mfma_f32_16x16x32_bf16 v[40:43], a[200:203], v[96:99], v[40:43]\n"
    "v_rcp_f32 v117, v117\n"
    "v_mfma_f32_16x16x32_bf16 v[40:43], a[216:219], v[100:103], v[40:43]\n"
    "buffer_load_dwordx4 v185, s[4:7], s28 offen lds\n"
    "v_mfma_f32_16x16x32_bf16 v[44:47], a[204:207], v[96:99], v[44:47]\n"
    "s_add_u32 m0, s53, 118784\n"
    "v_rcp_f32 v118, v118\n"
    "v_mfma_f32_16x16x32_bf16 v[44:47], a[220:223], v[100:103], v[44:47]\n"
    "s_waitcnt vmcnt(11) lgkmcnt(0)\n"
    "v_rcp_f32 v119, v119\n"
    "v_mfma_f32_16x16x32_bf16 v[32:35], a[224:227], v[104:107], v[32:35]\n"
    "s_barrier\n"
    "ds_read_b128 v[80:83], v138 offset:0\n"
    "v_mul_f32 v116, v116, v20\n"
    "v_mfma_f32_16x16x32_bf16 v[32:35], a[240:243], v[108:111], v[32:35]\n"
    "buffer_load_dwordx4 v185, s[4:7], s29 offen lds\n"
    "v_mfma_f32_16x16x32_bf16 v[36:39], a[228:231], v[104:107], v[36:39]\n"
    "s_add_u32 m0, s53, 122880\n"
    "ds_read_b128 v[84:87], v141 offset:0\n"
    "v_mul_f32 v117, v117, v21\n"
    "v_mul_f32 v118, v118, v22\n"
    "v_mfma_f32_16x16x32_bf16 v[36:39], a[244:247], v[108:111], v[36:39]\n"
    "v_mul_f32 v119, v119, v23\n"
    "v_mul_f32 v116, v116, v28\n"
    "v_mfma_f32_16x16x32_bf16 v[40:43], a[232:235], v[104:107], v[40:43]\n"
    "v_mul_f32 v117, v117, v29\n"
    "v_mul_f32 v118, v118, v30\n"
    "v_mfma_f32_16x16x32_bf16 v[40:43], a[248:251], v[108:111], v[40:43]\n"
    "buffer_load_dwordx4 v185, s[4:7], s30 offen lds\n"
    "v_mfma_f32_16x16x32_bf16 v[44:47], a[236:239], v[104:107], v[44:47]\n"
    "s_add_u32 m0, s53, 126976\n"
    "v_mul_f32 v119, v119, v31\n"
    "v_cvt_pk_bf16_f32 v122, v116, v117\n"
    "v_cvt_pk_bf16_f32 v123, v118, v119\n"
    "v_mfma_f32_16x16x32_bf16 v[44:47], a[252:255], v[108:111], v[44:47]\n"
    "s_waitcnt lgkmcnt(0)\n"
    "buffer_store_dwordx4 v[120:123], v186, s[20:23], s46 offen sc1\n"
    "s_nop 7\n"
    "v_pk_mul_f32 v[112:113], v[32:33], v[32:33]\n"
    "v_pk_mul_f32 v[114:115], v[34:35], v[34:35]\n"
    "v_pk_mul_f32 v[116:117], v[36:37], v[36:37]\n"
    "v_pk_mul_f32 v[118:119], v[38:39], v[38:39]\n"
    "v_pk_fma_f32 v[112:113], v[112:113], v[130:131], v[128:129]\n"
    "v_pk_fma_f32 v[114:115], v[114:115], v[130:131], v[128:129]\n"
    "v_pk_fma_f32 v[116:117], v[116:117], v[130:131], v[128:129]\n"
    "v_pk_fma_f32 v[118:119], v[118:119], v[130:131], v[128:129]\n"
    "v_pk_mul_f32 v[112:113], v[112:113], v[32:33]\n"
    "v_pk_mul_f32 v[114:115], v[114:115], v[34:35]\n"
    "v_pk_mul_f32 v[116:117], v[116:117], v[36:37]\n"
    "v_pk_mul_f32 v[118:119], v[118:119], v[38:39]\n"
    "v_exp_f32 v112, v112\n"
    "v_exp_f32 v113, v113\n"
    "v_exp_f32 v114, v114\n"
    "v_exp_f32 v115, v115\n"
    "v_exp_f32 v116, v116\n"
    "v_exp_f32 v117, v117\n"
    "v_exp_f32 v118, v118\n"
    "v_exp_f32 v119, v119\n"
    "v_pk_add_f32 v[112:113], v[112:113], 1.0 op_sel_hi:[1,0]\n"
    "v_pk_add_f32 v[114:115], v[114:115], 1.0 op_sel_hi:[1,0]\n"
    "v_pk_add_f32 v[116:117], v[116:117], 1.0 op_sel_hi:[1,0]\n"
    "v_pk_add_f32 v[118:119], v[118:119], 1.0 op_sel_hi:[1,0]\n"
    "v_rcp_f32 v112, v112\n"
    "v_rcp_f32 v113, v113\n"
    "v_rcp_f32 v114, v114\n"
    "v_rcp_f32 v115, v115\n"
    "v_rcp_f32 v116, v116\n"
    "v_rcp_f32 v117, v117\n"
    "v_rcp_f32 v118, v118\n"
    "v_rcp_f32 v119, v119\n"
    "v_pk_mul_f32 v[112:113], v[112:113], v[32:33]\n"
    "v_pk_mul_f32 v[114:115], v[114:115], v[34:35]\n"
    "v_pk_mul_f32 v[116:117], v[116:117], v[36:37]\n"
    "v_pk_mul_f32 v[118:119], v[118:119], v[38:39]\n"
    "v_pk_mul_f32 v[112:113], v[112:113], v[40:41]\n"
    "v_pk_mul_f32 v[114:115], v[114:115], v[42:43]\n"
    "v_pk_mul_f32 v[116:117], v[116:117], v[44:45]\n"
    "v_pk_mul_f32 v[118:119], v[118:119], v[46:47]\n"
    "v_cvt_pk_bf16_f32 v124, v112, v113\n"
    "v_cvt_pk_bf16_f32 v125, v114, v115\n"
    "v_cvt_pk_bf16_f32 v126, v116, v117\n"
    "v_cvt_pk_bf16_f32 v127, v118, v119\n"
    "buffer_store_dwordx4 v[124:127], v187, s[20:23], s46 offen sc1\n"
    "v_mfma_f32_16x16x32_bf16 v[16:19], a[0:3], v[80:83], 0\n"
    "ds_read_b128 v[88:91], v138 offset:4096\n"
    "s_lshr_b32 s99, s38, 2\n"
    "v_mfma_f32_16x16x32_bf16 v[16:19], a[16:19], v[84:87], v[16:19]\n"
    "s_waitcnt vmcnt(9)\n"
    "v_cmp_ne_u64 vcc, s[56:57], v[134:135]\n"
    "s_cbranch_vccnz L_spin0\n"
    "L_spin0_back:\n"
    "v_mfma_f32_16x16x32_bf16 v[20:23], a[4:7], v[80:83], 0\n"
    "ds_read_b128 v[92:95], v141 offset:4096\n"
    "s_xor_b32 s99, s99, 3\n"
    "v_mfma_f32_16x16x32_bf16 v[20:23], a[20:23], v[84:87], v[20:23]\n"
    "buffer_load_dwordx4 v185, s[4:7], s31 offen lds\n"
    "v_mfma_f32_16x16x32_bf16 v[24:27], a[8:11], v[80:83], 0\n"
    "s_add_u32 m0, s64, 131072\n"
    "s_lshl_b32 s93, s33, 2\n"
    "s_add_u32 s93, s93, s65\n"
    "v_mfma_f32_16x16x32_bf16 v[24:27], a[24:27], v[84:87], v[24:27]\n"
    "s_xor_b32 s92, s93, 2\n"
    "v_lshl_add_u32 v175, s36, 12, v136\n"
    "v_mfma_f32_16x16x32_bf16 v[28:31], a[12:15], v[80:83], 0\n"
    "v_xor_b32 v176, 0x1000, v175\n"
    "s_lshl_b32 s96, s93, 3\n"
    "v_mfma_f32_16x16x32_bf16 v[28:31], a[28:31], v[84:87], v[28:31]\n"
    "s_waitcnt lgkmcnt(0)\n"
    "buffer_load_dwordx4 v151, s[12:15], s88 offen lds\n"
    "v_mfma_f32_16x16x32_bf16 v[16:19], a[32:35], v[88:91], v[16:19]\n"
    "ds_read_b128 v[96:99], v138 offset:8192\n"
    "s_add_u32 s96, s96, s99\n"
    "v_mfma_f32_16x16x32_bf16 v[16:19], a[48:51], v[92:95], v[16:19]\n"
    "s_lshl_b32 s96, s96, 2\n"
    "s_add_u32 s96, s96, s79\n"
    "v_mfma_f32_16x16x32_bf16 v[20:23], a[36:39], v[88:91], v[20:23]\n"
    "ds_read_b128 v[100:103], v141 offset:8192\n"
    "v_lshl_add_u32 v136, s96, 10, v136\n"
    "v_mfma_f32_16x16x32_bf16 v[20:23], a[52:55], v[92:95], v[20:23]\n"
    "buffer_load_dwordx4 v152, s[12:15], s88 offen offset:1024 lds\n"
    "v_mfma_f32_16x16x32_bf16 v[24:27], a[40:43], v[88:91], v[24:27]\n"
    "v_xor_b32 v253, 65536, v136\n"
    "v_lshrrev_b32 v255, 2, v136\n"
    "v_mfma_f32_16x16x32_bf16 v[24:27], a[56:59], v[92:95], v[24:27]\n"
    "v_lshrrev_b32 v254, 2, v253\n"
    "s_mov_b32 s40, 9437184\n"
    "v_mfma_f32_16x16x32_bf16 v[28:31], a[44:47], v[88:91], v[28:31]\n"
    "s_mov_b32 s41, 10485760\n"
    "s_mov_b32 s72, 11534336\n"
    "v_mfma_f32_16x16x32_bf16 v[28:31], a[60:63], v[92:95], v[28:31]\n"
    "s_waitcnt lgkmcnt(0)\n"
    "buffer_load_dwordx4 v153, s[12:15], s88 offen offset:2048 lds\n"
    "v_mfma_f32_16x16x32_bf16 v[16:19], a[64:67], v[96:99], v[16:19]\n"
    "ds_read_b128 v[104:107], v138 offset:12288\n"
    "s_mov_b32 s43, 0xffff0000\n"
    "v_mfma_f32_16x16x32_bf16 v[16:19], a[80:83], v[100:103], v[16:19]\n"
    "s_lshl_b32 s96, s92, 16\n"
    "s_lshl_b32 s97, s79, 14\n"
    "v_mfma_f32_16x16x32_bf16 v[20:23], a[68:71], v[96:99], v[20:23]\n"
    "ds_read_b128 v[108:111], v141 offset:12288\n"
    "s_add_u32 s96, s96, s97\n"
    "v_mfma_f32_16x16x32_bf16 v[20:23], a[84:87], v[100:103], v[20:23]\n"
    "buffer_load_dwordx4 v154, s[12:15], s88 offen offset:3072 lds\n"
    "v_mfma_f32_16x16x32_bf16 v[24:27], a[72:75], v[96:99], v[24:27]\n"
    "s_add_u32 m0, s64, 147456\n"
    "s_lshl_b32 s97, s99, 7\n"
    "s_add_u32 s55, s96, s97\n"
    "v_mfma_f32_16x16x32_bf16 v[24:27], a[88:91], v[100:103], v[24:27]\n"
    "v_and_b32 v3, 15, v1\n"
    "v_lshlrev_b32 v3, 10, v3\n"
    "v_mfma_f32_16x16x32_bf16 v[28:31], a[76:79], v[96:99], v[28:31]\n"
    "v_bfe_u32 v4, v1, 4, 1\n"
    "v_lshl_add_u32 v3, v4, 5, v3\n"
    "v_mfma_f32_16x16x32_bf16 v[28:31], a[92:95], v[100:103], v[28:31]\n"
    "s_waitcnt lgkmcnt(0)\n"
    "buffer_load_dwordx4 v151, s[12:15], s89 offen lds\n"
    "v_mfma_f32_16x16x32_bf16 v[16:19], a[96:99], v[104:107], v[16:19]\n"
    "ds_read_b128 v[80:83], v138 offset:16384\n"
    "v_lshrrev_b32 v4, 5, v1\n"
    "v_mfma_f32_16x16x32_bf16 v[16:19], a[112:115], v[108:111], v[16:19]\n"
    "v_lshl_add_u32 v177, v4, 4, v3\n"
    "v_mfma_f32_16x16x32_bf16 v[20:23], a[100:103], v[104:107], v[20:23]\n"
    "ds_read_b128 v[84:87], v141 offset:16384\n"
    "v_mfma_f32_16x16x32_bf16 v[20:23], a[116:119], v[108:111], v[20:23]\n"
    "buffer_load_dwordx4 v152, s[12:15], s89 offen offset:1024 lds\n"
    "v_mfma_f32_16x16x32_bf16 v[24:27], a[104:107], v[104:107], v[24:27]\n"
    "v_mfma_f32_16x16x32_bf16 v[24:27], a[120:123], v[108:111], v[24:27]\n"
    "v_mfma_f32_16x16x32_bf16 v[28:31], a[108:111], v[104:107], v[28:31]\n"
    "v_mfma_f32_16x16x32_bf16 v[28:31], a[124:127], v[108:111], v[28:31]\n"
    "s_waitcnt lgkmcnt(0)\n"
    "buffer_load_dwordx4 v153, s[12:15], s89 offen offset:2048 lds\n"
    "v_mfma_f32_16x16x32_bf16 v[16:19], a[128:131], v[80:83], v[16:19]\n"
    "ds_read_b128 v[88:91], v138 offset:20480\n"
    "v_mfma_f32_16x16x32_bf16 v[16:19], a[144:147], v[84:87], v[16:19]\n"
    "v_mfma_f32_16x16x32_bf16 v[20:23], a[132:135], v[80:83], v[20:23]\n"
    "ds_read_b128 v[92:95], v141 offset:20480\n"
    "v_mfma_f32_16x16x32_bf16 v[20:23], a[148:151], v[84:87], v[20:23]\n"
    "buffer_load_dwordx4 v154, s[12:15], s89 offen offset:3072 lds\n"
    "v_mfma_f32_16x16x32_bf16 v[24:27], a[136:139], v[80:83], v[24:27]\n"
    "s_add_u32 m0, s64, 0\n"
    "v_mfma_f32_16x16x32_bf16 v[24:27], a[152:155], v[84:87], v[24:27]\n"
    "v_mfma_f32_16x16x32_bf16 v[28:31], a[140:143], v[80:83], v[28:31]\n"
    "v_mfma_f32_16x16x32_bf16 v[28:31], a[156:159], v[84:87], v[28:31]\n"
    "s_waitcnt lgkmcnt(0)\n"
    "v_mfma_f32_16x16x32_bf16 v[16:19], a[160:163], v[88:91], v[16:19]\n"
    "ds_read_b128 v[96:99], v138 offset:24576\n"
    "v_mfma_f32_16x16x32_bf16 v[16:19], a[176:179], v[92:95], v[16:19]\n"
    "v_mfma_f32_16x16x32_bf16 v[20:23], a[164:167], v[88:91], v[20:23]\n"
    "ds_read_b128 v[100:103], v141 offset:24576\n"
    "v_mfma_f32_16x16x32_bf16 v[20:23], a[180:183], v[92:95], v[20:23]\n"
    "buffer_load_dwordx4 v[188:191], v161, s[20:23], 0 offen sc1\n"
    "v_mfma_f32_16x16x32_bf16 v[24:27], a[168:171], v[88:91], v[24:27]\n"
    "v_mfma_f32_16x16x32_bf16 v[24:27], a[184:187], v[92:95], v[24:27]\n"
    "s_waitcnt vmcnt(10)\n"
    "buffer_store_dword v133, v137, s[20:23], s50 offen sc1\n"
    "v_mfma_f32_16x16x32_bf16 v[28:31], a[172:175], v[88:91], v[28:31]\n"
    "v_mfma_f32_16x16x32_bf16 v[28:31], a[188:191], v[92:95], v[28:31]\n"
    "s_waitcnt lgkmcnt(0)\n"
    "v_mfma_f32_16x16x32_bf16 v[16:19], a[192:195], v[96:99], v[16:19]\n"
    "ds_read_b128 v[104:107], v138 offset:28672\n"
    "v_mfma_f32_16x16x32_bf16 v[16:19], a[208:211], v[100:103], v[16:19]\n"
    "buffer_load_dwordx4 v[192:195], v162, s[20:23], 0 offen sc1\n"
    "v_mfma_f32_16x16x32_bf16 v[20:23], a[196:199], v[96:99], v[20:23]\n"
    "ds_read_b128 v[108:111], v141 offset:28672\n"
    "v_mfma_f32_16x16x32_bf16 v[20:23], a[212:215], v[100:103], v[20:23]\n"
    "v_mfma_f32_16x16x32_bf16 v[24:27], a[200:203], v[96:99], v[24:27]\n"
    "v_mfma_f32_16x16x32_bf16 v[24:27], a[216:219], v[100:103], v[24:27]\n"
    "buffer_load_dwordx4 v[196:199], v161, s[20:23], s58 offen sc1\n"
    "v_mfma_f32_16x16x32_bf16 v[28:31], a[204:207], v[96:99], v[28:31]\n"
    "v_mfma_f32_16x16x32_bf16 v[28:31], a[220:223], v[100:103], v[28:31]\n"
    "s_waitcnt vmcnt(12) lgkmcnt(0)\n"
    "v_mfma_f32_16x16x32_bf16 v[16:19], a[224:227], v[104:107], v[16:19]\n"
    "s_barrier\n"
    "ds_read_b128 v[80:83], v139 offset:32768\n"
    "v_mfma_f32_16x16x32_bf16 v[16:19], a[240:243], v[108:111], v[16:19]\n"
    "buffer_load_dwordx4 v151, s[12:15], s90 offen lds\n"
    "v_mfma_f32_16x16x32_bf16 v[20:23], a[228:231], v[104:107], v[20:23]\n"
    "ds_read_b128 v[84:87], v142 offset:32768\n"
    "v_mfma_f32_16x16x32_bf16 v[20:23], a[244:247], v[108:111], v[20:23]\n"
    "v_mfma_f32_16x16x32_bf16 v[24:27], a[232:235], v[104:107], v[24:27]\n"
    "v_mfma_f32_16x16x32_bf16 v[24:27], a[248:251], v[108:111], v[24:27]\n"
    "buffer_load_dwordx4 v152, s[12:15], s90 offen offset:1024 lds\n"
    "v_mfma_f32_16x16x32_bf16 v[28:31], a[236:239], v[104:107], v[28:31]\n"
    "v_mfma_f32_16x16x32_bf16 v[28:31], a[252:255], v[108:111], v[28:31]\n"
    "s_waitcnt lgkmcnt(0)\n"
    "v_mfma_f32_16x16x32_bf16 v[32:35], a[0:3], v[80:83], 0\n"
    "ds_read_b128 v[88:91], v139 offset:36864\n"
    "v_mfma_f32_16x16x32_bf16 v[32:35], a[16:19], v[84:87], v[32:35]\n"
    "buffer_load_dwordx4 v153, s[12:15], s90 offen offset:2048 lds\n"
    "v_mfma_f32_16x16x32_bf16 v[36:39], a[4:7], v[80:83], 0\n"
    "ds_read_b128 v[92:95], v142 offset:36864\n"
    "v_mfma_f32_16x16x32_bf16 v[36:39], a[20:23], v[84:87], v[36:39]\n"
    "v_mfma_f32_16x16x32_bf16 v[40:43], a[8:11], v[80:83], 0\n"
    "v_mfma_f32_16x16x32_bf16 v[40:43], a[24:27], v[84:87], v[40:43]\n"
    "buffer_load_dwordx4 v154, s[12:15], s90 offen offset:3072 lds\n"
    "v_mfma_f32_16x16x32_bf16 v[44:47], a[12:15], v[80:83], 0\n"
    "v_mul_f32 v112, v16, v16\n"
    "v_mul_f32 v113, v17, v17\n"
    "v_mul_f32 v114, v18, v18\n"
    "v_mfma_f32_16x16x32_bf16 v[44:47], a[28:31], v[84:87], v[44:47]\n"
    "s_waitcnt lgkmcnt(0)\n"
    "v_mul_f32 v115, v19, v19\n"
    "v_fma_f32 v112, v112, v130, v128\n"
    "v_fma_f32 v113, v113, v130, v128\n"
    "v_mfma_f32_16x16x32_bf16 v[32:35], a[32:35], v[88:91], v[32:35]\n"
    "ds_read_b128 v[96:99], v139 offset:40960\n"
    "v_fma_f32 v114, v114, v130, v128\n"
    "v_mfma_f32_16x16x32_bf16 v[32:35], a[48:51], v[92:95], v[32:35]\n"
    "buffer_load_dwordx4 v[200:203], v162, s[20:23], s58 offen sc1\n"
    "v_mfma_f32_16x16x32_bf16 v[36:39], a[36:39], v[88:91], v[36:39]\n"
    "ds_read_b128 v[100:103], v142 offset:40960\n"
    "v_fma_f32 v115, v115, v130, v128\n"
    "v_mul_f32 v112, v112, v16\n"
    "v_mfma_f32_16x16x32_bf16 v[36:39], a[52:55], v[92:95], v[36:39]\n"
    "L_stub2:\n"
    "s_cbranch_execz L_warm_back\n"
    "v_mul_f32 v113, v113, v17\n"
    "v_mul_f32 v114, v114, v18\n"
    "v_mul_f32 v115, v115, v19\n"
    "v_mfma_f32_16x16x32_bf16 v[40:43], a[40:43], v[88:91], v[40:43]\n"
    "v_exp_f32 v112, v112\n"
    "v_mfma_f32_16x16x32_bf16 v[40:43], a[56:59], v[92:95], v[40:43]\n"
    "buffer_load_dwordx4 v[204:207], v161, s[20:23], s59 offen sc1\n"
    "v_mfma_f32_16x16x32_bf16 v[44:47], a[44:47], v[88:91], v[44:47]\n"
    "v_exp_f32 v113, v113\n"
    "v_mfma_f32_16x16x32_bf16 v[44:47], a[60:63], v[92:95], v[44:47]\n"
    "s_waitcnt lgkmcnt(0)\n"
    "v_exp_f32 v114, v114\n"
    "v_mfma_f32_16x16x32_bf16 v[32:35], a[64:67], v[96:99], v[32:35]\n"
    "ds_read_b128 v[104:107], v139 offset:45056\n"
    "v_mfma_f32_16x16x32_bf16 v[32:35], a[80:83], v[100:103], v[32:35]\n"
    "buffer_load_dwordx4 v[208:211], v162, s[20:23], s59 offen sc1\n"
    "v_mfma_f32_16x16x32_bf16 v[36:39], a[68:71], v[96:99], v[36:39]\n"
    "ds_read_b128 v[108:111], v142 offset:45056\n"
    "v_exp_f32 v115, v115\n"
    "v_mfma_f32_16x16x32_bf16 v[36:39], a[84:87], v[100:103], v[36:39]\n"
    "v_add_f32 v112, 1.0, v112\n"
    "v_add_f32 v113, 1.0, v113\n"
    "v_add_f32 v114, 1.0, v114\n"
    "v_mfma_f32_16x16x32_bf16 v[40:43], a[72:75], v[96:99], v[40:43]\n"
    "v_add_f32 v115, 1.0, v115\n"
    "v_mfma_f32_16x16x32_bf16 v[40:43], a[88:91], v[100:103], v[40:43]\n"
    "v_rcp_f32 v112, v112\n"
    "v_mfma_f32_16x16x32_bf16 v[44:47], a[76:79], v[96:99], v[44:47]\n"
    "v_rcp_f32 v113, v113\n"
    "v_mfma_f32_16x16x32_bf16 v[44:47], a[92:95], v[100:103], v[44:47]\n"
    "s_waitcnt lgkmcnt(0)\n"
    "v_rcp_f32 v114, v114\n"
    "v_mfma_f32_16x16x32_bf16 v[32:35], a[96:99], v[104:107], v[32:35]\n"
    "ds_read_b128 v[80:83], v139 offset:49152\n"
    "v_mfma_f32_16x16x32_bf16 v[32:35], a[112:115], v[108:111], v[32:35]\n"
    "v_rcp_f32 v115, v115\n"
    "v_mfma_f32_16x16x32_bf16 v[36:39], a[100:103], v[104:107], v[36:39]\n"
    "ds_read_b128 v[84:87], v142 offset:49152\n"
    "v_mul_f32 v112, v112, v16\n"
    "v_mfma_f32_16x16x32_bf16 v[36:39], a[116:119], v[108:111], v[36:39]\n"
    "v_mul_f32 v113, v113, v17\n"
    "v_mul_f32 v114, v114, v18\n"
    "v_mfma_f32_16x16x32_bf16 v[40:43], a[104:107], v[104:107], v[40:43]\n"
    "v_mul_f32 v115, v115, v19\n"
    "v_mul_f32 v112, v112, v24\n"
    "v_mfma_f32_16x16x32_bf16 v[40:43], a[120:123], v[108:111], v[40:43]\n"
    "buffer_load_dwordx4 v[212:215], v161, s[20:23], s60 offen sc1\n"
    "v_mfma_f32_16x16x32_bf16 v[44:47], a[108:111], v[104:107], v[44:47]\n"
    "v_mul_f32 v113, v113, v25\n"
    "v_mul_f32 v114, v114, v26\n"
    "v_mfma_f32_16x16x32_bf16 v[44:47], a[124:127], v[108:111], v[44:47]\n"
    "s_waitcnt lgkmcnt(0)\n"
    "v_mul_f32 v115, v115, v27\n"
    "v_cvt_pk_bf16_f32 v120, v112, v113\n"
    "v_mfma_f32_16x16x32_bf16 v[32:35], a[128:131], v[80:83], v[32:35]\n"
    "ds_read_b128 v[88:91], v139 offset:53248\n"
    "v_cvt_pk_bf16_f32 v121, v114, v115\n"
    "v_mfma_f32_16x16x32_bf16 v[32:35], a[144:147], v[84:87], v[32:35]\n"
    "buffer_load_dwordx4 v[216:219], v162, s[20:23], s60 offen sc1\n"
    "v_mfma_f32_16x16x32_bf16 v[36:39], a[132:135], v[80:83], v[36:39]\n"
    "ds_read_b128 v[92:95], v142 offset:53248\n"
    "v_mul_f32 v116, v20, v20\n"
    "v_mfma_f32_16x16x32_bf16 v[36:39], a[148:151], v[84:87], v[36:39]\n"
    "buffer_load_dwordx2 v[134:135], v158, s[20:23], 0 offen sc1\n"
    "v_mfma_f32_16x16x32_bf16 v[40:43], a[136:139], v[80:83], v[40:43]\n"
    "v_mul_f32 v117, v21, v21\n"
    "v_mul_f32 v118, v22, v22\n"
    "v_mul_f32 v119, v23, v23\n"
    "v_mfma_f32_16x16x32_bf16 v[40:43], a[152:155], v[84:87], v[40:43]\n"
    "v_fma_f32 v116, v116, v130, v128\n"
    "v_fma_f32 v117, v117, v130, v128\n"
    "v_fma_f32 v118, v118, v130, v128\n"
    "v_mfma_f32_16x16x32_bf16 v[44:47], a[140:143], v[80:83], v[44:47]\n"
    "v_fma_f32 v119, v119, v130, v128\n"
    "v_mul_f32 v116, v116, v20\n"
    "v_mfma_f32_16x16x32_bf16 v[44:47], a[156:159], v[84:87], v[44:47]\n"
    "s_waitcnt lgkmcnt(0)\n"
    "buffer_load_dwordx4 v[220:223], v161, s[20:23], s61 offen sc1\n"
    "v_mfma_f32_16x16x32_bf16 v[32:35], a[160:163], v[88:91], v[32:35]\n"
    "ds_read_b128 v[96:99], v139 offset:57344\n"
    "v_mul_f32 v117, v117, v21\n"
    "v_mfma_f32_16x16x32_bf16 v[32:35], a[176:179], v[92:95], v[32:35]\n"
    "v_mul_f32 v118, v118, v22\n"
    "v_mul_f32 v119, v119, v23\n"
    "v_mfma_f32_16x16x32_bf16 v[36:39], a[164:167], v[88:91], v[36:39]\n"
    "ds_read_b128 v[100:103], v142 offset:57344\n"
    "v_mfma_f32_16x16x32_bf16 v[36:39], a[180:183], v[92:95], v[36:39]\n"
    "v_exp_f32 v116, v116\n"
    "v_mfma_f32_16x16x32_bf16 v[40:43], a[168:171], v[88:91], v[40:43]\n"
    "v_exp_f32 v117, v117\n"
    "v_mfma_f32_16x16x32_bf16 v[40:43], a[184:187], v[92:95], v[40:43]\n"
    "v_exp_f32 v118, v118\n"
    "v_mfma_f32_16x16x32_bf16 v[44:47], a[172:175], v[88:91], v[44:47]\n"
    "v_exp_f32 v119, v119\n"
    "v_mfma_f32_16x16x32_bf16 v[44:47], a[188:191], v[92:95], v[44:47]\n"
    "s_waitcnt lgkmcnt(0)\n"
    "buffer_load_dwordx4 v[224:227], v162, s[20:23], s61 offen sc1\n"
    "v_mfma_f32_16x16x32_bf16 v[32:35], a[192:195], v[96:99], v[32:35]\n"
    "ds_read_b128 v[104:107], v139 offset:61440\n"
    "v_add_f32 v116, 1.0, v116\n"
    "v_mfma_f32_16x16x32_bf16 v[32:35], a[208:211], v[100:103], v[32:35]\n"
    "v_add_f32 v117, 1.0, v117\n"
    "v_add_f32 v118, 1.0, v118\n"
    "v_mfma_f32_16x16x32_bf16 v[36:39], a[196:199], v[96:99], v[36:39]\n"
    "ds_read_b128 v[108:111], v142 offset:61440\n"
    "v_add_f32 v119, 1.0, v119\n"
    "v_mfma_f32_16x16x32_bf16 v[36:39], a[212:215], v[100:103], v[36:39]\n"
    "buffer_load_dwordx4 v[228:231], v161, s[20:23], s62 offen sc1\n"
    "v_mfma_f32_16x16x32_bf16 v[40:43], a[200:203], v[96:99], v[40:43]\n"
    "v_rcp_f32 v116, v116\n"
    "v_mfma_f32_16x16x32_bf16 v[40:43], a[216:219], v[100:103], v[40:43]\n"
    "v_rcp_f32 v117, v117\n"
    "v_mfma_f32_16x16x32_bf16 v[44:47], a[204:207], v[96:99], v[44:47]\n"
    "v_rcp_f32 v118, v118\n"
    "v_mfma_f32_16x16x32_bf16 v[44:47], a[220:223], v[100:103], v[44:47]\n"
    "s_waitcnt lgkmcnt(0)\n"
    "buffer_load_dwordx4 v[232:235], v162, s[20:23], s62 offen sc1\n"
    "v_mfma_f32_16x16x32_bf16 v[32:35], a[224:227], v[104:107], v[32:35]\n"
    "v_rcp_f32 v119, v119\n"
    "v_mul_f32 v116, v116, v20\n"
    "v_mfma_f32_16x16x32_bf16 v[32:35], a[240:243], v[108:111], v[32:35]\n"
    "v_mul_f32 v117, v117, v21\n"
    "v_mul_f32 v118, v118, v22\n"
    "v_mfma_f32_16x16x32_bf16 v[36:39], a[228:231], v[104:107], v[36:39]\n"
    "v_mul_f32 v119, v119, v23\n"
    "v_mul_f32 v116, v116, v28\n"
    "v_mfma_f32_16x16x32_bf16 v[36:39], a[244:247], v[108:111], v[36:39]\n"
    "buffer_load_dwordx4 v[236:239], v161, s[20:23], s63 offen sc1\n"
    "v_mfma_f32_16x16x32_bf16 v[40:43], a[232:235], v[104:107], v[40:43]\n"
    "v_mul_f32 v117, v117, v29\n"
    "v_mul_f32 v118, v118, v30\n"
    "v_mfma_f32_16x16x32_bf16 v[40:43], a[248:251], v[108:111], v[40:43]\n"
    "v_mul_f32 v119, v119, v31\n"
    "v_cvt_pk_bf16_f32 v122, v116, v117\n"
    "v_mfma_f32_16x16x32_bf16 v[44:47], a[236:239], v[104:107], v[44:47]\n"
    "v_cvt_pk_bf16_f32 v123, v118, v119\n"
    "v_mfma_f32_16x16x32_bf16 v[44:47], a[252:255], v[108:111], v[44:47]\n"
    "buffer_store_dwordx4 v[120:123], v186, s[20:23], s47 offen sc1\n"
    "ds_read_b64_tr_b16 a[32:33], v163 offset:32768\n"
    "ds_read_b64_tr_b16 a[34:35], v163 offset:34816\n"
    "ds_read_b64_tr_b16 a[36:37], v166 offset:32768\n"
    "ds_read_b64_tr_b16 a[38:39], v166 offset:34816\n"
    "ds_read_b64_tr_b16 a[40:41], v169 offset:32768\n"
    "ds_read_b64_tr_b16 a[42:43], v169 offset:34816\n"
    "ds_read_b64_tr_b16 a[44:45], v172 offset:32768\n"
    "ds_read_b64_tr_b16 a[46:47], v172 offset:34816\n"
    "v_pk_mul_f32 v[112:113], v[32:33], v[32:33]\n"
    "v_pk_mul_f32 v[114:115], v[34:35], v[34:35]\n"
    "v_pk_mul_f32 v[116:117], v[36:37], v[36:37]\n"
    "v_pk_mul_f32 v[118:119], v[38:39], v[38:39]\n"
    "v_pk_fma_f32 v[112:113], v[112:113], v[130:131], v[128:129]\n"
    "v_pk_fma_f32 v[114:115], v[114:115], v[130:131], v[128:129]\n"
    "v_pk_fma_f32 v[116:117], v[116:117], v[130:131], v[128:129]\n"
    "v_pk_fma_f32 v[118:119], v[118:119], v[130:131], v[128:129]\n"
    "v_pk_mul_f32 v[112:113], v[112:113], v[32:33]\n"
    "v_pk_mul_f32 v[114:115], v[114:115], v[34:35]\n"
    "v_pk_mul_f32 v[116:117], v[116:117], v[36:37]\n"
    "v_pk_mul_f32 v[118:119], v[118:119], v[38:39]\n"
    "v_exp_f32 v112, v112\n"
    "v_exp_f32 v113, v113\n"
    "v_exp_f32 v114, v114\n"
    "v_exp_f32 v115, v115\n"
    "v_exp_f32 v116, v116\n"
    "v_exp_f32 v117, v117\n"
    "v_exp_f32 v118, v118\n"
    "v_exp_f32 v119, v119\n"
    "v_pk_add_f32 v[112:113], v[112:113], 1.0 op_sel_hi:[1,0]\n"
    "v_pk_add_f32 v[114:115], v[114:115], 1.0 op_sel_hi:[1,0]\n"
    "v_pk_add_f32 v[116:117], v[116:117], 1.0 op_sel_hi:[1,0]\n"
    "v_pk_add_f32 v[118:119], v[118:119], 1.0 op_sel_hi:[1,0]\n"
    "v_rcp_f32 v112, v112\n"
    "v_rcp_f32 v113, v113\n"
    "v_rcp_f32 v114, v114\n"
    "v_rcp_f32 v115, v115\n"
    "v_rcp_f32 v116, v116\n"
    "v_rcp_f32 v117, v117\n"
    "v_rcp_f32 v118, v118\n"
    "v_rcp_f32 v119, v119\n"
    "v_pk_mul_f32 v[112:113], v[112:113], v[32:33]\n"
    "v_pk_mul_f32 v[114:115], v[114:115], v[34:35]\n"
    "v_pk_mul_f32 v[116:117], v[116:117], v[36:37]\n"
    "v_pk_mul_f32 v[118:119], v[118:119], v[38:39]\n"
    "v_pk_mul_f32 v[112:113], v[112:113], v[40:41]\n"
    "v_pk_mul_f32 v[114:115], v[114:115], v[42:43]\n"
    "v_pk_mul_f32 v[116:117], v[116:117], v[44:45]\n"
    "v_pk_mul_f32 v[118:119], v[118:119], v[46:47]\n"
    "v_cvt_pk_bf16_f32 v124, v112, v113\n"
    "v_cvt_pk_bf16_f32 v125, v114, v115\n"
    "v_cvt_pk_bf16_f32 v126, v116, v117\n"
    "v_cvt_pk_bf16_f32 v127, v118, v119\n"
    "buffer_store_dwordx4 v[124:127], v187, s[20:23], s47 offen sc1\n"
    "s_waitcnt vmcnt(20) lgkmcnt(6)\n"
    "v_mfma_f32_16x16x32_bf16 v[80:83], a[32:35], v[188:191], 0\n"
    "s_waitcnt vmcnt(18)\n"
    "ds_read_b64_tr_b16 a[48:49], v163 offset:40960\n"
    "ds_read_b64_tr_b16 a[50:51], v163 offset:43008\n"
    "v_mfma_f32_16x16x32_bf16 v[96:99], a[32:35], v[192:195], 0\n"
    "s_waitcnt lgkmcnt(6)\n"
    "buffer_load_dwordx4 v[240:243], v162, s[20:23], s63 offen sc1\n"
    "v_mfma_f32_16x16x32_bf16 v[84:87], a[36:39], v[188:191], 0\n"
    "s_waitcnt vmcnt(8)\n"
    "v_cmp_ne_u64 vcc, s[56:57], v[134:135]\n"
    "s_cbranch_vccnz L_spin1\n"
    "L_spin1_back:\n"
    "v_add_u32 v161, s42, v161\n"
    "v_add_u32 v162, s42, v162\n"
    "v_mfma_f32_16x16x32_bf16 v[100:103], a[36:39], v[192:195], 0\n"
    "s_waitcnt lgkmcnt(4)\n"
    "ds_read_b64_tr_b16 a[52:53], v166 offset:40960\n"
    "ds_read_b64_tr_b16 a[54:55], v166 offset:43008\n"
    "v_mfma_f32_16x16x32_bf16 v[88:91], a[40:43], v[188:191], 0\n"
    "buffer_load_dwordx4 v[48:51], v161, s[20:23], 0 offen sc1\n"
    "v_mfma_f32_16x16x32_bf16 v[104:107], a[40:43], v[192:195], 0\n"
    "s_waitcnt lgkmcnt(4)\n"
    "ds_read_b64_tr_b16 a[56:57], v169 offset:40960\n"
    "ds_read_b64_tr_b16 a[58:59], v169 offset:43008\n"
    "v_mfma_f32_16x16x32_bf16 v[92:95], a[44:47], v[188:191], 0\n"
    "s_barrier\n"
    "v_mfma_f32_16x16x32_bf16 v[108:111], a[44:47], v[192:195], 0\n"
    "s_waitcnt lgkmcnt(4)\n"
    "ds_read_b64_tr_b16 a[60:61], v172 offset:40960\n"
    "ds_read_b64_tr_b16 a[62:63], v172 offset:43008\n"
    "v_mfma_f32_16x16x32_bf16 v[80:83], a[48:51], v[196:199], v[80:83]\n"
    "buffer_load_dwordx4 v[52:55], v161, s[20:23], s58 offen sc1\n"
    "v_mfma_f32_16x16x32_bf16 v[96:99], a[48:51], v[200:203], v[96:99]\n"
    "s_waitcnt lgkmcnt(4)\n"
    "ds_read_b64_tr_b16 a[64:65], v163 offset:49152\n"
    "ds_read_b64_tr_b16 a[66:67], v163 offset:51200\n"
    "v_mfma_f32_16x16x32_bf16 v[84:87], a[52:55], v[196:199], v[84:87]\n"
    "ds_read_b64_tr_b16 a[68:69], v166 offset:49152\n"
    "ds_read_b64_tr_b16 a[70:71], v166 offset:51200\n"
    "v_mfma_f32_16x16x32_bf16 v[100:103], a[52:55], v[200:203], v[100:103]\n"
    "s_waitcnt lgkmcnt(6)\n"
    "ds_read_b64_tr_b16 a[72:73], v169 offset:49152\n"
    "ds_read_b64_tr_b16 a[74:75], v169 offset:51200\n"
    "v_mfma_f32_16x16x32_bf16 v[88:91], a[56:59], v[196:199], v[88:91]\n"
    "buffer_load_dwordx4 v[56:59], v161, s[20:23], s59 offen sc1\n"
    "v_mfma_f32_16x16x32_bf16 v[104:107], a[56:59], v[200:203], v[104:107]\n"
    "s_waitcnt lgkmcnt(6)\n"
    "ds_read_b64_tr_b16 a[76:77], v172 offset:49152\n"
    "ds_read_b64_tr_b16 a[78:79], v172 offset:51200\n"
    "v_mfma_f32_16x16x32_bf16 v[92:95], a[60:63], v[196:199], v[92:95]\n"
    "ds_read_b64_tr_b16 a[80:81], v163 offset:57344\n"
    "ds_read_b64_tr_b16 a[82:83], v163 offset:59392\n"
    "v_mfma_f32_16x16x32_bf16 v[108:111], a[60:63], v[200:203], v[108:111]\n"
    "s_waitcnt lgkmcnt(8)\n"
    "ds_read_b64_tr_b16 a[84:85], v166 offset:57344\n"
    "ds_read_b64_tr_b16 a[86:87], v166 offset:59392\n"
    "v_mfma_f32_16x16x32_bf16 v[80:83], a[64:67], v[204:207], v[80:83]\n"
    "buffer_load_dwordx4 v[60:63], v161, s[20:23], s60 offen sc1\n"
    "v_mfma_f32_16x16x32_bf16 v[96:99], a[64:67], v[208:211], v[96:99]\n"
    "s_waitcnt lgkmcnt(8)\n"
    "ds_read_b64_tr_b16 a[88:89], v169 offset:57344\n"
    "ds_read_b64_tr_b16 a[90:91], v169 offset:59392\n"
    "v_mfma_f32_16x16x32_bf16 v[84:87], a[68:71], v[204:207], v[84:87]\n"
    "ds_read_b64_tr_b16 a[92:93], v172 offset:57344\n"
    "ds_read_b64_tr_b16 a[94:95], v172 offset:59392\n"
    "v_mfma_f32_16x16x32_bf16 v[100:103], a[68:71], v[208:211], v[100:103]\n"
    "s_waitcnt lgkmcnt(10)\n"
    "ds_read_b64_tr_b16 a[96:97], v164 offset:0\n"
    "ds_read_b64_tr_b16 a[98:99], v164 offset:2048\n"
    "v_mfma_f32_16x16x32_bf16 v[88:91], a[72:75], v[204:207], v[88:91]\n"
    "buffer_load_dwordx4 v[64:67], v161, s[20:23], s61 offen sc1\n"
    "v_mfma_f32_16x16x32_bf16 v[104:107], a[72:75], v[208:211], v[104:107]\n"
    "s_waitcnt lgkmcnt(10)\n"
    "ds_read_b64_tr_b16 a[100:101], v167 offset:0\n"
    "ds_read_b64_tr_b16 a[102:103], v167 offset:2048\n"
    "v_mfma_f32_16x16x32_bf16 v[92:95], a[76:79], v[204:207], v[92:95]\n"
    "ds_read_b64_tr_b16 a[104:105], v170 offset:0\n"
    "ds_read_b64_tr_b16 a[106:107], v170 offset:2048\n"
    "v_mfma_f32_16x16x32_bf16 v[108:111], a[76:79], v[208:211], v[108:111]\n"
    "s_waitcnt lgkmcnt(12)\n"
    "ds_read_b64_tr_b16 a[108:109], v173 offset:0\n"
    "ds_read_b64_tr_b16 a[110:111], v173 offset:2048\n"
    "v_mfma_f32_16x16x32_bf16 v[80:83], a[80:83], v[212:215], v[80:83]\n"
    "buffer_load_dwordx4 v[68:71], v161, s[20:23], s62 offen sc1\n"
    "v_mfma_f32_16x16x32_bf16 v[96:99], a[80:83], v[216:219], v[96:99]\n"
    "s_waitcnt lgkmcnt(12)\n"
    "ds_read_b64_tr_b16 a[112:113], v164 offset:8192\n"
    "ds_read_b64_tr_b16 a[114:115], v164 offset:10240\n"
    "v_mfma_f32_16x16x32_bf16 v[84:87], a[84:87], v[212:215], v[84:87]\n"
    "ds_read_b64_tr_b16 a[116:117], v167 offset:8192\n"
    "ds_read_b64_tr_b16 a[118:119], v167 offset:10240\n"
    "v_mfma_f32_16x16x32_bf16 v[100:103], a[84:87], v[216:219], v[100:103]\n"
    "s_waitcnt lgkmcnt(14)\n"
    "ds_read_b64_tr_b16 a[120:121], v170 offset:8192\n"
    "ds_read_b64_tr_b16 a[122:123], v170 offset:10240\n"
    "v_mfma_f32_16x16x32_bf16 v[88:91], a[88:91], v[212:215], v[88:91]\n"
    "buffer_load_dwordx4 v[72:75], v161, s[20:23], s63 offen sc1\n"
    "v_mfma_f32_16x16x32_bf16 v[104:107], a[88:91], v[216:219], v[104:107]\n"
    "s_waitcnt lgkmcnt(14)\n"
    "ds_read_b64_tr_b16 a[124:125], v173 offset:8192\n"
    "ds_read_b64_tr_b16 a[126:127], v173 offset:10240\n"
    "v_mfma_f32_16x16x32_bf16 v[92:95], a[92:95], v[212:215], v[92:95]\n"
    "ds_read_b64_tr_b16 a[128:129], v164 offset:16384\n"
    "ds_read_b64_tr_b16 a[130:131], v164 offset:18432\n"
    "v_mfma_f32_16x16x32_bf16 v[108:111], a[92:95], v[216:219], v[108:111]\n"
    "s_waitcnt vmcnt(14) lgkmcnt(14)\n"
    "ds_read_b64_tr_b16 a[132:133], v167 offset:16384\n"
    "ds_read_b64_tr_b16 a[134:135], v167 offset:18432\n"
    "v_mfma_f32_16x16x32_bf16 v[80:83], a[96:99], v[220:223], v[80:83]\n"
    "s_waitcnt vmcnt(13)\n"
    "buffer_load_dwordx4 v[76:79], v162, s[20:23], 0 offen sc1\n"
    "v_mfma_f32_16x16x32_bf16 v[96:99], a[96:99], v[224:227], v[96:99]\n"
    "buffer_load_dwordx2 v[134:135], v159, s[20:23], 0 offen sc1\n"
    "v_mfma_f32_16x16x32_bf16 v[84:87], a[100:103], v[220:223], v[84:87]\n"
    "ds_read_b64_tr_b16 a[136:137], v170 offset:16384\n"
    "ds_read_b64_tr_b16 a[138:139], v170 offset:18432\n"
    "v_mfma_f32_16x16x32_bf16 v[100:103], a[100:103], v[224:227], v[100:103]\n"
    "s_waitcnt lgkmcnt(14)\n"
    "ds_read_b64_tr_b16 a[140:141], v173 offset:16384\n"
    "ds_read_b64_tr_b16 a[142:143], v173 offset:18432\n"
    "v_mfma_f32_16x16x32_bf16 v[88:91], a[104:107], v[220:223], v[88:91]\n"
    "ds_read_b64_tr_b16 a[144:145], v164 offset:24576\n"
    "ds_read_b64_tr_b16 a[146:147], v164 offset:26624\n"
    "v_mfma_f32_16x16x32_bf16 v[104:107], a[104:107], v[224:227], v[104:107]\n"
    "s_waitcnt vmcnt(10)\n"
    "buffer_store_dword v133, v137, s[20:23], s51 offen sc1\n"
    "v_mfma_f32_16x16x32_bf16 v[92:95], a[108:111], v[220:223], v[92:95]\n"
    "ds_read_b64_tr_b16 a[160:161], v165 offset:0\n"
    "ds_read_b64_tr_b16 a[162:163], v165 offset:2048\n"
    "v_mfma_f32_16x16x32_bf16 v[108:111], a[108:111], v[224:227], v[108:111]\n"
    "s_waitcnt lgkmcnt(14)\n"
    "ds_read_b64_tr_b16 a[176:177], v165 offset:8192\n"
    "ds_read_b64_tr_b16 a[178:179], v165 offset:10240\n"
    "v_mfma_f32_16x16x32_bf16 v[80:83], a[112:115], v[228:231], v[80:83]\n"
    "ds_read_b64_tr_b16 a[192:193], v165 offset:16384\n"
    "ds_read_b64_tr_b16 a[194:195], v165 offset:18432\n"
    "v_mfma_f32_16x16x32_bf16 v[96:99], a[112:115], v[232:235], v[96:99]\n"
    "ds_read_b64_tr_b16 a[208:209], v165 offset:24576\n"
    "ds_read_b64_tr_b16 a[210:211], v165 offset:26624\n"
    "v_mfma_f32_16x16x32_bf16 v[84:87], a[116:119], v[228:231], v[84:87]\n"
    "ds_read_b64_tr_b16 a[224:225], v163 offset:0\n"
    "ds_read_b64_tr_b16 a[226:227], v163 offset:2048\n"
    "v_mfma_f32_16x16x32_bf16 v[100:103], a[116:119], v[232:235], v[100:103]\n"
    "ds_read_b64_tr_b16 a[240:241], v163 offset:8192\n"
    "ds_read_b64_tr_b16 a[242:243], v163 offset:10240\n"
    "v_mfma_f32_16x16x32_bf16 v[88:91], a[120:123], v[228:231], v[88:91]\n"
    "buffer_load_dwordx4 a[0:3], v162, s[20:23], s58 offen sc1\n"
    "v_mfma_f32_16x16x32_bf16 v[104:107], a[120:123], v[232:235], v[104:107]\n"
    "s_waitcnt lgkmcnt(14)\n"
    "ds_read_b64_tr_b16 a[148:149], v167 offset:24576\n"
    "ds_read_b64_tr_b16 a[150:151], v167 offset:26624\n"
    "v_mfma_f32_16x16x32_bf16 v[92:95], a[124:127], v[228:231], v[92:95]\n"
    "ds_read_b64_tr_b16 a[164:165], v168 offset:0\n"
    "ds_read_b64_tr_b16 a[166:167], v168 offset:2048\n"
    "v_mfma_f32_16x16x32_bf16 v[108:111], a[124:127], v[232:235], v[108:111]\n"
    "ds_read_b64_tr_b16 a[180:181], v168 offset:8192\n"
    "ds_read_b64_tr_b16 a[182:183], v168 offset:10240\n"
    "v_mfma_f32_16x16x32_bf16 v[80:83], a[128:131], v[236:239], v[80:83]\n"
    "s_waitcnt vmcnt(11)\n"
    "buffer_load_dwordx4 a[4:7], v162, s[20:23], s59 offen sc1\n"
    "v_mfma_f32_16x16x32_bf16 v[96:99], a[128:131], v[240:243], v[96:99]\n"
    "ds_read_b64_tr_b16 a[196:197], v168 offset:16384\n"
    "ds_read_b64_tr_b16 a[198:199], v168 offset:18432\n"
    "v_mfma_f32_16x16x32_bf16 v[84:87], a[132:135], v[236:239], v[84:87]\n"
    "ds_read_b64_tr_b16 a[212:213], v168 offset:24576\n"
    "ds_read_b64_tr_b16 a[214:215], v168 offset:26624\n"
    "v_mfma_f32_16x16x32_bf16 v[100:103], a[132:135], v[240:243], v[100:103]\n"
    "ds_read_b64_tr_b16 a[228:229], v166 offset:0\n"
    "ds_read_b64_tr_b16 a[230:231], v166 offset:2048\n"
    "v_mfma_f32_16x16x32_bf16 v[88:91], a[136:139], v[236:239], v[88:91]\n"
    "buffer_load_dwordx4 a[8:11], v162, s[20:23], s60 offen sc1\n"
    "v_mfma_f32_16x16x32_bf16 v[104:107], a[136:139], v[240:243], v[104:107]\n"
    "ds_read_b64_tr_b16 a[244:245], v166 offset:8192\n"
    "ds_read_b64_tr_b16 a[246:247], v166 offset:10240\n"
    "v_mfma_f32_16x16x32_bf16 v[92:95], a[140:143], v[236:239], v[92:95]\n"
    "ds_read_b64_tr_b16 a[152:153], v170 offset:24576\n"
    "ds_read_b64_tr_b16 a[154:155], v170 offset:26624\n"
    "v_mfma_f32_16x16x32_bf16 v[108:111], a[140:143], v[240:243], v[108:111]\n"
    "s_waitcnt vmcnt(12) lgkmcnt(14)\n"
    "ds_read_b64_tr_b16 a[168:169], v171 offset:0\n"
    "ds_read_b64_tr_b16 a[170:171], v171 offset:2048\n"
    "v_mfma_f32_16x16x32_bf16 v[80:83], a[144:147], v[48:51], v[80:83]\n"
    "s_waitcnt vmcnt(11)\n"
    "buffer_load_dwordx4 a[12:15], v162, s[20:23], s61 offen sc1\n"
    "v_mfma_f32_16x16x32_bf16 v[80:83], a[160:163], v[52:55], v[80:83]\n"
    "s_waitcnt vmcnt(11)\n"
    "ds_read_b64_tr_b16 a[184:185], v171 offset:8192\n"
    "ds_read_b64_tr_b16 a[186:187], v171 offset:10240\n"
    "v_mfma_f32_16x16x32_bf16 v[80:83], a[176:179], v[56:59], v[80:83]\n"
    "s_waitcnt vmcnt(10)\n"
    "ds_read_b64_tr_b16 a[200:201], v171 offset:16384\n"
    "ds_read_b64_tr_b16 a[202:203], v171 offset:18432\n"
    "v_mfma_f32_16x16x32_bf16 v[80:83], a[192:195], v[60:63], v[80:83]\n"
    "s_waitcnt vmcnt(9)\n"
    "ds_read_b64_tr_b16 a[216:217], v171 offset:24576\n"
    "ds_read_b64_tr_b16 a[218:219], v171 offset:26624\n"
    "v_mfma_f32_16x16x32_bf16 v[80:83], a[208:211], v[64:67], v[80:83]\n"
    "buffer_load_dwordx4 a[16:19], v162, s[20:23], s62 offen sc1\n"
    "ds_read_b64_tr_b16 a[232:233], v169 offset:0\n"
    "s_waitcnt vmcnt(9)\n"
    "v_mfma_f32_16x16x32_bf16 v[80:83], a[224:227], v[68:71], v[80:83]\n"
    "ds_read_b64_tr_b16 a[234:235], v169 offset:2048\n"
    "ds_read_b64_tr_b16 a[248:249], v169 offset:8192\n"
    "ds_read_b64_tr_b16 a[250:251], v169 offset:10240\n"
    "ds_read_b64_tr_b16 a[156:157], v173 offset:24576\n"
    "ds_read_b64_tr_b16 a[158:159], v173 offset:26624\n"
    "ds_read_b64_tr_b16 a[172:173], v174 offset:0\n"
    "ds_read_b64_tr_b16 a[174:175], v174 offset:2048\n"
    "ds_read_b64_tr_b16 a[188:189], v174 offset:8192\n"
    "ds_read_b64_tr_b16 a[190:191], v174 offset:10240\n"
    "ds_read_b64_tr_b16 a[204:205], v174 offset:16384\n"
    "ds_read_b64_tr_b16 a[206:207], v174 offset:18432\n"
    "ds_read_b64_tr_b16 a[220:221], v174 offset:24576\n"
    "buffer_load_dwordx4 a[20:23], v162, s[20:23], s63 offen sc1\n"
    "s_waitcnt vmcnt(9)\n"
    "v_mfma_f32_16x16x32_bf16 v[80:83], a[240:243], v[72:75], v[80:83]\n"
    "ds_read_b64_tr_b16 a[222:223], v174 offset:26624\n"
    "ds_read_b64_tr_b16 a[236:237], v172 offset:0\n"
    "v_mfma_f32_16x16x32_bf16 v[84:87], a[148:151], v[48:51], v[84:87]\n"
    "s_waitcnt lgkmcnt(14)\n"
    "ds_read_b64_tr_b16 a[238:239], v172 offset:2048\n"
    "ds_read_b64_tr_b16 a[252:253], v172 offset:8192\n"
    "v_mfma_f32_16x16x32_bf16 v[84:87], a[164:167], v[52:55], v[84:87]\n"
    "ds_read_b64_tr_b16 a[254:255], v172 offset:10240\n"
    "v_mfma_f32_16x16x32_bf16 v[84:87], a[180:183], v[56:59], v[84:87]\n"
    "v_mfma_f32_16x16x32_bf16 v[84:87], a[196:199], v[60:63], v[84:87]\n"
    "v_mfma_f32_16x16x32_bf16 v[84:87], a[212:215], v[64:67], v[84:87]\n"
    "v_mfma_f32_16x16x32_bf16 v[84:87], a[228:231], v[68:71], v[84:87]\n"
    "v_mfma_f32_16x16x32_bf16 v[84:87], a[244:247], v[72:75], v[84:87]\n"
    "v_mfma_f32_16x16x32_bf16 v[88:91], a[152:155], v[48:51], v[88:91]\n"
    "ds_write_b128 v175, v[80:83] offset:16384\n"
    "v_mfma_f32_16x16x32_bf16 v[88:91], a[168:171], v[52:55], v[88:91]\n"
    "s_waitcnt vmcnt(7)\n"
    "v_cmp_ne_u64 vcc, s[56:57], v[134:135]\n"
    "s_cbranch_vccnz L_spin2\n"
    "L_spin2_back:\n"
    "v_add_u32 v161, s54, v161\n"
    "v_add_u32 v162, s54, v162\n"
    "v_mfma_f32_16x16x32_bf16 v[88:91], a[184:187], v[56:59], v[88:91]\n"
    "buffer_load_dwordx4 v[188:191], v161, s[20:23], 0 offen sc1\n"
    "v_mfma_f32_16x16x32_bf16 v[88:91], a[200:203], v[60:63], v[88:91]\n"
    "v_mfma_f32_16x16x32_bf16 v[88:91], a[216:219], v[64:67], v[88:91]\n"
    "s_waitcnt lgkmcnt(14)\n"
    "v_mfma_f32_16x16x32_bf16 v[88:91], a[232:235], v[68:71], v[88:91]\n"
    "v_mfma_f32_16x16x32_bf16 v[88:91], a[248:251], v[72:75], v[88:91]\n"
    "s_waitcnt lgkmcnt(13)\n"
    "buffer_load_dwordx4 v[192:195], v161, s[20:23], s58 offen sc1\n"
    "v_mfma_f32_16x16x32_bf16 v[92:95], a[156:159], v[48:51], v[92:95]\n"
    "s_waitcnt lgkmcnt(11)\n"
    "ds_write_b128 v175, v[84:87] offset:17408\n"
    "v_mfma_f32_16x16x32_bf16 v[92:95], a[172:175], v[52:55], v[92:95]\n"
    "s_waitcnt lgkmcnt(10)\n"
    "v_mfma_f32_16x16x32_bf16 v[92:95], a[188:191], v[56:59], v[92:95]\n"
    "s_waitcnt lgkmcnt(8)\n"
    "v_mfma_f32_16x16x32_bf16 v[92:95], a[204:207], v[60:63], v[92:95]\n"
    "s_waitcnt lgkmcnt(6)\n"
    "buffer_load_dwordx4 v[196:199], v161, s[20:23], s59 offen sc1\n"
    "v_mfma_f32_16x16x32_bf16 v[92:95], a[220:223], v[64:67], v[92:95]\n"
    "s_waitcnt lgkmcnt(4)\n"
    "v_mfma_f32_16x16x32_bf16 v[92:95], a[236:239], v[68:71], v[92:95]\n"
    "s_waitcnt lgkmcnt(2)\n"
    "v_mfma_f32_16x16x32_bf16 v[92:95], a[252:255], v[72:75], v[92:95]\n"
    "v_mfma_f32_16x16x32_bf16 v[96:99], a[144:147], v[76:79], v[96:99]\n"
    "ds_write_b128 v175, v[88:91] offset:18432\n"
    "buffer_load_dwordx4 v[200:203], v161, s[20:23], s60 offen sc1\n"
    "s_waitcnt vmcnt(9)\n"
    "v_mfma_f32_16x16x32_bf16 v[96:99], a[160:163], a[0:3], v[96:99]\n"
    "buffer_load_dwordx4 v[204:207], v161, s[20:23], s61 offen sc1\n"
    "s_waitcnt vmcnt(9)\n"
    "v_mfma_f32_16x16x32_bf16 v[96:99], a[176:179], a[4:7], v[96:99]\n"
    "buffer_load_dwordx4 v[208:211], v161, s[20:23], s62 offen sc1\n"
    "s_waitcnt vmcnt(9)\n"
    "v_mfma_f32_16x16x32_bf16 v[96:99], a[192:195], a[8:11], v[96:99]\n"
    "buffer_load_dwordx4 v[212:215], v161, s[20:23], s63 offen sc1\n"
    "s_waitcnt vmcnt(9)\n"
    "v_mfma_f32_16x16x32_bf16 v[96:99], a[208:211], a[12:15], v[96:99]\n"
    "buffer_load_dwordx4 v[216:219], v162, s[20:23], 0 offen sc1\n"
    "s_waitcnt vmcnt(9)\n"
    "v_mfma_f32_16x16x32_bf16 v[96:99], a[224:227], a[16:19], v[96:99]\n"
    "buffer_load_dwordx4 v[220:223], v162, s[20:23], s58 offen sc1\n"
    "s_waitcnt vmcnt(9)\n"
    "v_mfma_f32_16x16x32_bf16 v[96:99], a[240:243], a[20:23], v[96:99]\n"
    "buffer_load_dwordx4 v[224:227], v162, s[20:23], s59 offen sc1\n"
    "v_mfma_f32_16x16x32_bf16 v[100:103], a[148:151], v[76:79], v[100:103]\n"
    "ds_write_b128 v175, v[92:95] offset:19456\n"
    "v_mfma_f32_16x16x32_bf16 v[100:103], a[164:167], a[0:3], v[100:103]\n"
    "v_mfma_f32_16x16x32_bf16 v[100:103], a[180:183], a[4:7], v[100:103]\n"
    "v_mfma_f32_16x16x32_bf16 v[100:103], a[196:199], a[8:11], v[100:103]\n"
    "buffer_load_dwordx4 v[228:231], v162, s[20:23], s60 offen sc1\n"
    "v_mfma_f32_16x16x32_bf16 v[100:103], a[212:215], a[12:15], v[100:103]\n"
    "s_waitcnt lgkmcnt(0)\n"
    "v_mfma_f32_16x16x32_bf16 v[100:103], a[228:231], a[16:19], v[100:103]\n"
    "s_barrier\n"
    "ds_read_b128 v[16:19], v176 offset:16384\n"
    "ds_read_b128 v[20:23], v176 offset:17408\n"
    "v_mfma_f32_16x16x32_bf16 v[100:103], a[244:247], a[20:23], v[100:103]\n"
    "ds_read_b128 v[24:27], v176 offset:18432\n"
    "ds_read_b128 v[28:31], v176 offset:19456\n"
    "v_mfma_f32_16x16x32_bf16 v[104:107], a[152:155], v[76:79], v[104:107]\n"
    "buffer_load_dwordx4 v[232:235], v162, s[20:23], s61 offen sc1\n"
    "v_mfma_f32_16x16x32_bf16 v[104:107], a[168:171], a[0:3], v[104:107]\n"
    "v_mfma_f32_16x16x32_bf16 v[104:107], a[184:187], a[4:7], v[104:107]\n"
    "v_mfma_f32_16x16x32_bf16 v[104:107], a[200:203], a[8:11], v[104:107]\n"
    "v_mfma_f32_16x16x32_bf16 v[104:107], a[216:219], a[12:15], v[104:107]\n"
    "buffer_load_dwordx4 v[236:239], v162, s[20:23], s62 offen sc1\n"
    "v_mfma_f32_16x16x32_bf16 v[104:107], a[232:235], a[16:19], v[104:107]\n"
    "s_waitcnt lgkmcnt(0)\n"
    "v_add_f32 v16, v16, v96\n"
    "v_mfma_f32_16x16x32_bf16 v[104:107], a[248:251], a[20:23], v[104:107]\n"
    "v_add_f32 v17, v17, v97\n"
    "v_add_f32 v18, v18, v98\n"
    "v_mfma_f32_16x16x32_bf16 v[108:111], a[156:159], v[76:79], v[108:111]\n"
    "v_add_f32 v19, v19, v99\n"
    "v_cvt_pk_bf16_f32 v16, v16, v17\n"
    "v_mfma_f32_16x16x32_bf16 v[108:111], a[172:175], a[0:3], v[108:111]\n"
    "buffer_load_dwordx4 v[240:243], v162, s[20:23], s63 offen sc1\n"
    "v_mfma_f32_16x16x32_bf16 v[108:111], a[188:191], a[4:7], v[108:111]\n"
    "v_cvt_pk_bf16_f32 v17, v18, v19\n"
    "v_add3_u32 v15, v132, v16, v17\n"
    "v_mfma_f32_16x16x32_bf16 v[108:111], a[204:207], a[8:11], v[108:111]\n"
    "v_add_f32 v20, v20, v100\n"
    "v_add_f32 v21, v21, v101\n"
    "v_mfma_f32_16x16x32_bf16 v[108:111], a[220:223], a[12:15], v[108:111]\n"
    "v_add_f32 v22, v22, v102\n"
    "v_add_f32 v23, v23, v103\n"
    "v_mfma_f32_16x16x32_bf16 v[108:111], a[236:239], a[16:19], v[108:111]\n"
    "v_cvt_pk_bf16_f32 v18, v20, v21\n"
    "v_cvt_pk_bf16_f32 v19, v22, v23\n"
    "v_mfma_f32_16x16x32_bf16 v[108:111], a[252:255], a[20:23], v[108:111]\n"
    "s_waitcnt vmcnt(13)\n"
    "v_add3_u32 v15, v15, v18, v19\n"
    "v_mfma_f32_16x16x32_bf16 v[80:83], a[32:35], v[188:191], 0\n"
    "s_waitcnt vmcnt(12)\n"
    "buffer_load_dwordx2 v[134:135], v160, s[20:23], 0 offen sc1\n"
    "v_mfma_f32_16x16x32_bf16 v[80:83], a[48:51], v[192:195], v[80:83]\n"
    "s_waitcnt vmcnt(12)\n"
    "buffer_store_dwordx4 v[16:19], v136, s[20:23], s40 offen sc1\n"
    "v_mfma_f32_16x16x32_bf16 v[80:83], a[64:67], v[196:199], v[80:83]\n"
    "s_waitcnt vmcnt(12)\n"
    "v_add_f32 v24, v24, v104\n"
    "v_add_f32 v25, v25, v105\n"
    "v_mfma_f32_16x16x32_bf16 v[80:83], a[80:83], v[200:203], v[80:83]\n"
    "s_waitcnt vmcnt(11)\n"
    "v_add_f32 v26, v26, v106\n"
    "v_add_f32 v27, v27, v107\n"
    "v_mfma_f32_16x16x32_bf16 v[80:83], a[96:99], v[204:207], v[80:83]\n"
    "s_waitcnt vmcnt(10)\n"
    "v_cvt_pk_bf16_f32 v20, v24, v25\n"
    "v_cvt_pk_bf16_f32 v21, v26, v27\n"
    "v_mfma_f32_16x16x32_bf16 v[80:83], a[112:115], v[208:211], v[80:83]\n"
    "s_waitcnt vmcnt(9)\n"
    "v_add3_u32 v15, v15, v20, v21\n"
    "v_add_f32 v28, v28, v108\n"
    "v_mfma_f32_16x16x32_bf16 v[80:83], a[128:131], v[212:215], v[80:83]\n"
    "v_add_f32 v29, v29, v109\n"
    "v_add_f32 v30, v30, v110\n"
    "v_mfma_f32_16x16x32_bf16 v[84:87], a[36:39], v[188:191], 0\n"
    "v_add_f32 v31, v31, v111\n"
    "v_cvt_pk_bf16_f32 v22, v28, v29\n"
    "v_mfma_f32_16x16x32_bf16 v[84:87], a[52:55], v[192:195], v[84:87]\n"
    "v_cvt_pk_bf16_f32 v23, v30, v31\n"
    "v_add3_u32 v15, v15, v22, v23\n"
    "v_mfma_f32_16x16x32_bf16 v[84:87], a[68:71], v[196:199], v[84:87]\n"
    "buffer_store_dwordx4 v[20:23], v136, s[20:23], s41 offen sc1\n"
    "v_mfma_f32_16x16x32_bf16 v[84:87], a[84:87], v[200:203], v[84:87]\n"
    "buffer_store_dword v15, v255, s[20:23], s72 offen sc1\n"
    "v_mfma_f32_16x16x32_bf16 v[84:87], a[100:103], v[204:207], v[84:87]\n"
    "v_add_u32 v175, 0x10000, v175\n"
    "v_add_u32 v176, 0x10000, v176\n"
    "v_mfma_f32_16x16x32_bf16 v[84:87], a[116:119], v[208:211], v[84:87]\n"
    "v_mfma_f32_16x16x32_bf16 v[84:87], a[132:135], v[212:215], v[84:87]\n"
    "v_mfma_f32_16x16x32_bf16 v[88:91], a[40:43], v[188:191], 0\n"
    "v_mfma_f32_16x16x32_bf16 v[88:91], a[56:59], v[192:195], v[88:91]\n"
    "v_mfma_f32_16x16x32_bf16 v[88:91], a[72:75], v[196:199], v[88:91]\n"
    "v_mfma_f32_16x16x32_bf16 v[88:91], a[88:91], v[200:203], v[88:91]\n"
    "v_mfma_f32_16x16x32_bf16 v[88:91], a[104:107], v[204:207], v[88:91]\n"
    "v_mfma_f32_16x16x32_bf16 v[88:91], a[120:123], v[208:211], v[88:91]\n"
    "v_mfma_f32_16x16x32_bf16 v[88:91], a[136:139], v[212:215], v[88:91]\n"
    "v_mfma_f32_16x16x32_bf16 v[92:95], a[44:47], v[188:191], 0\n"
    "v_mfma_f32_16x16x32_bf16 v[92:95], a[60:63], v[192:195], v[92:95]\n"
    "v_mfma_f32_16x16x32_bf16 v[92:95], a[76:79], v[196:199], v[92:95]\n"
    "v_mfma_f32_16x16x32_bf16 v[92:95], a[92:95], v[200:203], v[92:95]\n"
    "v_mfma_f32_16x16x32_bf16 v[92:95], a[108:111], v[204:207], v[92:95]\n"
    "v_mfma_f32_16x16x32_bf16 v[92:95], a[124:127], v[208:211], v[92:95]\n"
    "v_mfma_f32_16x16x32_bf16 v[92:95], a[140:143], v[212:215], v[92:95]\n"
    "s_waitcnt vmcnt(10)\n"
    "v_mfma_f32_16x16x32_bf16 v[96:99], a[32:35], v[216:219], 0\n"
    "s_waitcnt vmcnt(9)\n"
    "v_mfma_f32_16x16x32_bf16 v[96:99], a[48:51], v[220:223], v[96:99]\n"
    "s_waitcnt vmcnt(8)\n"
    "v_mfma_f32_16x16x32_bf16 v[96:99], a[64:67], v[224:227], v[96:99]\n"
    "s_waitcnt vmcnt(7)\n"
    "v_mfma_f32_16x16x32_bf16 v[96:99], a[80:83], v[228:231], v[96:99]\n"
    "s_waitcnt vmcnt(6)\n"
    "v_mfma_f32_16x16x32_bf16 v[96:99], a[96:99], v[232:235], v[96:99]\n"
    "s_waitcnt vmcnt(5)\n"
    "v_mfma_f32_16x16x32_bf16 v[96:99], a[112:115], v[236:239], v[96:99]\n"
    "s_waitcnt vmcnt(4)\n"
    "v_mfma_f32_16x16x32_bf16 v[96:99], a[128:131], v[240:243], v[96:99]\n"
    "s_waitcnt vmcnt(3)\n"
    "v_cmp_ne_u64 vcc, s[56:57], v[134:135]\n"
    "s_cbranch_vccnz L_spin3\n"
    "L_spin3_back:\n"
    "v_add_u32 v161, s42, v161\n"
    "v_add_u32 v162, s42, v162\n"
    "v_mfma_f32_16x16x32_bf16 v[100:103], a[36:39], v[216:219], 0\n"
    "L_stub3:\n"
    "s_cbranch_execz L_warm_back\n"
    "buffer_load_dwordx4 v[48:51], v161, s[20:23], 0 offen sc1\n"
    "v_mfma_f32_16x16x32_bf16 v[100:103], a[52:55], v[220:223], v[100:103]\n"
    "v_mfma_f32_16x16x32_bf16 v[100:103], a[68:71], v[224:227], v[100:103]\n"
    "v_mfma_f32_16x16x32_bf16 v[100:103], a[84:87], v[228:231], v[100:103]\n"
    "v_mfma_f32_16x16x32_bf16 v[100:103], a[100:103], v[232:235], v[100:103]\n"
    "buffer_load_dwordx4 v[52:55], v161, s[20:23], s58 offen sc1\n"
    "v_mfma_f32_16x16x32_bf16 v[100:103], a[116:119], v[236:239], v[100:103]\n"
    "v_mfma_f32_16x16x32_bf16 v[100:103], a[132:135], v[240:243], v[100:103]\n"
    "v_mfma_f32_16x16x32_bf16 v[104:107], a[40:43], v[216:219], 0\n"
    "v_mfma_f32_16x16x32_bf16 v[104:107], a[56:59], v[220:223], v[104:107]\n"
    "buffer_load_dwordx4 v[56:59], v161, s[20:23], s59 offen sc1\n"
    "v_mfma_f32_16x16x32_bf16 v[104:107], a[72:75], v[224:227], v[104:107]\n"
    "v_mfma_f32_16x16x32_bf16 v[104:107], a[88:91], v[228:231], v[104:107]\n"
    "v_mfma_f32_16x16x32_bf16 v[104:107], a[104:107], v[232:235], v[104:107]\n"
    "v_mfma_f32_16x16x32_bf16 v[104:107], a[120:123], v[236:239], v[104:107]\n"
    "buffer_load_dwordx4 v[60:63], v161, s[20:23], s60 offen sc1\n"
    "v_mfma_f32_16x16x32_bf16 v[104:107], a[136:139], v[240:243], v[104:107]\n"
    "v_mfma_f32_16x16x32_bf16 v[108:111], a[44:47], v[216:219], 0\n"
    "v_mfma_f32_16x16x32_bf16 v[108:111], a[60:63], v[220:223], v[108:111]\n"
    "v_mfma_f32_16x16x32_bf16 v[108:111], a[76:79], v[224:227], v[108:111]\n"
    "buffer_load_dwordx4 v[64:67], v161, s[20:23], s61 offen sc1\n"
    "v_mfma_f32_16x16x32_bf16 v[108:111], a[92:95], v[228:231], v[108:111]\n"
    "v_mfma_f32_16x16x32_bf16 v[108:111], a[108:111], v[232:235], v[108:111]\n"
    "v_mfma_f32_16x16x32_bf16 v[108:111], a[124:127], v[236:239], v[108:111]\n"
    "v_mfma_f32_16x16x32_bf16 v[108:111], a[140:143], v[240:243], v[108:111]\n"
    "buffer_load_dwordx4 v[68:71], v161, s[20:23], s62 offen sc1\n"
    "buffer_load_dwordx4 v[72:75], v161, s[20:23], s63 offen sc1\n"
    "buffer_load_dwordx4 v[76:79], v162, s[20:23], 0 offen sc1\n"
    "buffer_load_dwordx4 a[0:3], v162, s[20:23], s58 offen sc1\n"
    "buffer_load_dwordx4 a[4:7], v162, s[20:23], s59 offen sc1\n"
    "s_waitcnt vmcnt(9)\n"
    "v_mfma_f32_16x16x32_bf16 v[80:83], a[144:147], v[48:51], v[80:83]\n"
    "buffer_load_dwordx4 a[8:11], v162, s[20:23], s60 offen sc1\n"
    "v_mfma_f32_16x16x32_bf16 v[84:87], a[148:151], v[48:51], v[84:87]\n"
    "v_mfma_f32_16x16x32_bf16 v[88:91], a[152:155], v[48:51], v[88:91]\n"
    "v_mfma_f32_16x16x32_bf16 v[92:95], a[156:159], v[48:51], v[92:95]\n"
    "s_waitcnt vmcnt(9)\n"
    "v_mfma_f32_16x16x32_bf16 v[80:83], a[160:163], v[52:55], v[80:83]\n"
    "buffer_load_dwordx4 a[12:15], v162, s[20:23], s61 offen sc1\n"
    "v_mfma_f32_16x16x32_bf16 v[84:87], a[164:167], v[52:55], v[84:87]\n"
    "v_mfma_f32_16x16x32_bf16 v[88:91], a[168:171], v[52:55], v[88:91]\n"
    "v_mfma_f32_16x16x32_bf16 v[92:95], a[172:175], v[52:55], v[92:95]\n"
    "s_waitcnt vmcnt(9)\n"
    "v_mfma_f32_16x16x32_bf16 v[80:83], a[176:179], v[56:59], v[80:83]\n"
    "buffer_load_dwordx4 a[16:19], v162, s[20:23], s62 offen sc1\n"
    "v_mfma_f32_16x16x32_bf16 v[84:87], a[180:183], v[56:59], v[84:87]\n"
    "v_mfma_f32_16x16x32_bf16 v[88:91], a[184:187], v[56:59], v[88:91]\n"
    "v_mfma_f32_16x16x32_bf16 v[92:95], a[188:191], v[56:59], v[92:95]\n"
    "s_waitcnt vmcnt(9)\n"
    "v_mfma_f32_16x16x32_bf16 v[80:83], a[192:195], v[60:63], v[80:83]\n"
    "buffer_load_dwordx4 a[20:23], v162, s[20:23], s63 offen sc1\n"
    "v_mfma_f32_16x16x32_bf16 v[84:87], a[196:199], v[60:63], v[84:87]\n"
    "v_mfma_f32_16x16x32_bf16 v[88:91], a[200:203], v[60:63], v[88:91]\n"
    "v_mfma_f32_16x16x32_bf16 v[92:95], a[204:207], v[60:63], v[92:95]\n"
    "s_waitcnt vmcnt(9)\n"
    "v_mfma_f32_16x16x32_bf16 v[80:83], a[208:211], v[64:67], v[80:83]\n"
    "buffer_load_dwordx4 v[32:35], v253, s[20:23], s40 offen sc1\n"
    "v_mfma_f32_16x16x32_bf16 v[84:87], a[212:215], v[64:67], v[84:87]\n"
    "v_mfma_f32_16x16x32_bf16 v[88:91], a[216:219], v[64:67], v[88:91]\n"
    "v_mfma_f32_16x16x32_bf16 v[92:95], a[220:223], v[64:67], v[92:95]\n"
    "s_waitcnt vmcnt(9)\n"
    "v_mfma_f32_16x16x32_bf16 v[80:83], a[224:227], v[68:71], v[80:83]\n"
    "buffer_load_dwordx4 v[36:39], v253, s[20:23], s41 offen sc1\n"
    "v_mfma_f32_16x16x32_bf16 v[84:87], a[228:231], v[68:71], v[84:87]\n"
    "v_mfma_f32_16x16x32_bf16 v[88:91], a[232:235], v[68:71], v[88:91]\n"
    "v_mfma_f32_16x16x32_bf16 v[92:95], a[236:239], v[68:71], v[92:95]\n"
    "s_waitcnt vmcnt(9)\n"
    "v_mfma_f32_16x16x32_bf16 v[80:83], a[240:243], v[72:75], v[80:83]\n"
    "buffer_load_dword v40, v254, s[20:23], s72 offen sc1\n"
    "v_mfma_f32_16x16x32_bf16 v[84:87], a[244:247], v[72:75], v[84:87]\n"
    "v_mfma_f32_16x16x32_bf16 v[88:91], a[248:251], v[72:75], v[88:91]\n"
    "v_mfma_f32_16x16x32_bf16 v[92:95], a[252:255], v[72:75], v[92:95]\n"
    "s_waitcnt vmcnt(9)\n"
    "v_mfma_f32_16x16x32_bf16 v[96:99], a[144:147], v[76:79], v[96:99]\n"
    "v_mfma_f32_16x16x32_bf16 v[100:103], a[148:151], v[76:79], v[100:103]\n"
    "v_mfma_f32_16x16x32_bf16 v[104:107], a[152:155], v[76:79], v[104:107]\n"
    "v_mfma_f32_16x16x32_bf16 v[108:111], a[156:159], v[76:79], v[108:111]\n"
    "s_waitcnt vmcnt(8)\n"
    "v_mfma_f32_16x16x32_bf16 v[96:99], a[160:163], a[0:3], v[96:99]\n"
    "ds_write_b128 v175, v[80:83] offset:32768\n"
    "v_mfma_f32_16x16x32_bf16 v[100:103], a[164:167], a[0:3], v[100:103]\n"
    "v_mfma_f32_16x16x32_bf16 v[104:107], a[168:171], a[0:3], v[104:107]\n"
    "ds_write_b128 v175, v[84:87] offset:33792\n"
    "v_mfma_f32_16x16x32_bf16 v[108:111], a[172:175], a[0:3], v[108:111]\n"
    "s_waitcnt vmcnt(7)\n"
    "v_mfma_f32_16x16x32_bf16 v[96:99], a[176:179], a[4:7], v[96:99]\n"
    "ds_write_b128 v175, v[88:91] offset:34816\n"
    "v_mfma_f32_16x16x32_bf16 v[100:103], a[180:183], a[4:7], v[100:103]\n"
    "v_mfma_f32_16x16x32_bf16 v[104:107], a[184:187], a[4:7], v[104:107]\n"
    "ds_write_b128 v175, v[92:95] offset:35840\n"
    "v_mfma_f32_16x16x32_bf16 v[108:111], a[188:191], a[4:7], v[108:111]\n"
    "s_waitcnt vmcnt(6)\n"
    "v_mfma_f32_16x16x32_bf16 v[96:99], a[192:195], a[8:11], v[96:99]\n"
    "v_mfma_f32_16x16x32_bf16 v[100:103], a[196:199], a[8:11], v[100:103]\n"
    "v_mfma_f32_16x16x32_bf16 v[104:107], a[200:203], a[8:11], v[104:107]\n"
    "s_waitcnt lgkmcnt(0)\n"
    "v_mfma_f32_16x16x32_bf16 v[108:111], a[204:207], a[8:11], v[108:111]\n"
    "s_waitcnt vmcnt(5)\n"
    "s_barrier\n"
    "ds_read_b128 v[16:19], v176 offset:32768\n"
    "ds_read_b128 v[20:23], v176 offset:33792\n"
    "v_mfma_f32_16x16x32_bf16 v[96:99], a[208:211], a[12:15], v[96:99]\n"
    "ds_read_b128 v[24:27], v176 offset:34816\n"
    "ds_read_b128 v[28:31], v176 offset:35840\n"
    "v_mfma_f32_16x16x32_bf16 v[100:103], a[212:215], a[12:15], v[100:103]\n"
    "v_mfma_f32_16x16x32_bf16 v[104:107], a[216:219], a[12:15], v[104:107]\n"
    "v_mfma_f32_16x16x32_bf16 v[108:111], a[220:223], a[12:15], v[108:111]\n"
    "s_waitcnt vmcnt(4)\n"
    "v_mfma_f32_16x16x32_bf16 v[96:99], a[224:227], a[16:19], v[96:99]\n"
    "v_mfma_f32_16x16x32_bf16 v[100:103], a[228:231], a[16:19], v[100:103]\n"
    "v_mfma_f32_16x16x32_bf16 v[104:107], a[232:235], a[16:19], v[104:107]\n"
    "v_mfma_f32_16x16x32_bf16 v[108:111], a[236:239], a[16:19], v[108:111]\n"
    "s_waitcnt vmcnt(3)\n"
    "v_mfma_f32_16x16x32_bf16 v[96:99], a[240:243], a[20:23], v[96:99]\n"
    "v_mfma_f32_16x16x32_bf16 v[100:103], a[244:247], a[20:23], v[100:103]\n"
    "v_mfma_f32_16x16x32_bf16 v[104:107], a[248:251], a[20:23], v[104:107]\n"
    "v_mfma_f32_16x16x32_bf16 v[108:111], a[252:255], a[20:23], v[108:111]\n"
    "L_tail:\n"
    "s_waitcnt lgkmcnt(0)\n"
    "s_nop 4\n"
    "v_pk_add_f32 v[96:97], v[96:97], v[16:17]\n"
    "v_pk_add_f32 v[98:99], v[98:99], v[18:19]\n"
    "v_pk_add_f32 v[100:101], v[100:101], v[20:21]\n"
    "v_pk_add_f32 v[102:103], v[102:103], v[22:23]\n"
    "v_pk_add_f32 v[104:105], v[104:105], v[24:25]\n"
    "v_pk_add_f32 v[106:107], v[106:107], v[26:27]\n"
    "v_pk_add_f32 v[108:109], v[108:109], v[28:29]\n"
    "v_pk_add_f32 v[110:111], v[110:111], v[30:31]\n"
    "s_waitcnt vmcnt(0)\n"
    "v_add3_u32 v112, v132, v32, v33\n"
    "v_add3_u32 v113, v34, v35, v36\n"
    "v_add3_u32 v112, v112, v37, v38\n"
    "v_add3_u32 v112, v112, v113, v39\n"
    "v_cmp_ne_u32 vcc, v112, v40\n"
    "s_cbranch_vccnz L_pspin\n"
    "L_pspin_back:\n"
    "v_lshlrev_b32 v16, 16, v32\n"
    "v_and_b32 v17, s43, v32\n"
    "v_lshlrev_b32 v18, 16, v33\n"
    "v_and_b32 v19, s43, v33\n"
    "v_lshlrev_b32 v20, 16, v34\n"
    "v_and_b32 v21, s43, v34\n"
    "v_lshlrev_b32 v22, 16, v35\n"
    "v_and_b32 v23, s43, v35\n"
    "v_pk_add_f32 v[96:97], v[96:97], v[16:17]\n"
    "v_pk_add_f32 v[98:99], v[98:99], v[18:19]\n"
    "v_pk_add_f32 v[100:101], v[100:101], v[20:21]\n"
    "v_pk_add_f32 v[102:103], v[102:103], v[22:23]\n"
    "v_cvt_pk_bf16_f32 v120, v96, v97\n"
    "v_cvt_pk_bf16_f32 v121, v98, v99\n"
    "v_cvt_pk_bf16_f32 v122, v100, v101\n"
    "v_cvt_pk_bf16_f32 v123, v102, v103\n"
    "s_nop 0\n"
    "v_permlane16_swap_b32 v120, v122\n"
    "v_permlane16_swap_b32 v121, v123\n"
    "buffer_store_dwordx4 v[120:123], v177, s[16:19], s55 offen\n"
    "v_lshlrev_b32 v24, 16, v36\n"
    "v_and_b32 v25, s43, v36\n"
    "v_lshlrev_b32 v26, 16, v37\n"
    "v_and_b32 v27, s43, v37\n"
    "v_lshlrev_b32 v28, 16, v38\n"
    "v_and_b32 v29, s43, v38\n"
    "v_lshlrev_b32 v30, 16, v39\n"
    "v_and_b32 v31, s43, v39\n"
    "v_pk_add_f32 v[104:105], v[104:105], v[24:25]\n"
    "v_pk_add_f32 v[106:107], v[106:107], v[26:27]\n"
    "v_pk_add_f32 v[108:109], v[108:109], v[28:29]\n"
    "v_pk_add_f32 v[110:111], v[110:111], v[30:31]\n"
    "v_cvt_pk_bf16_f32 v124, v104, v105\n"
    "v_cvt_pk_bf16_f32 v125, v106, v107\n"
    "v_cvt_pk_bf16_f32 v126, v108, v109\n"
    "v_cvt_pk_bf16_f32 v127, v110, v111\n"
    "s_nop 0\n"
    "v_permlane16_swap_b32 v124, v126\n"
    "v_permlane16_swap_b32 v125, v127\n"
    "buffer_store_dwordx4 v[124:127], v177, s[16:19], s55 offen offset:64\n"
    "s_endpgm\n"
    "L_idle2:\n"
    "s_waitcnt lgkmcnt(0)\n"
    "s_mov_b32 s44, s98\n"
    "s_mov_b32 s45, s96\n"
    "s_load_dwordx2 s[12:13], %0, 24\n"
    "s_load_dwordx2 s[16:17], %0, 32\n"
    "s_load_dwordx2 s[56:57], s[20:21], 0\n"
    "s_mov_b32 s14, 1835008\n"
    "s_mov_b32 s15, 0x20000\n"
    "s_mov_b32 s18, 2097152\n"
    "s_mov_b32 s19, 0x20000\n"
    "s_mov_b32 s22, 13631488\n"
    "s_mov_b32 s23, 0x20000\n"
    "s_mov_b32 s6, 2097152\n"
    "s_lshr_b32 s99, s38, 2\n"
    "s_xor_b32 s99, s99, 3\n"
    "s_xor_b32 s65, s37, 2\n"
    "s_xor_b32 s92, s65, 1\n"
    "s_lshl_b32 s64, s36, 12\n"
    "s_mul_i32 s96, s65, 14\n"
    "s_add_u32 s96, s96, s36\n"
    "s_lshl_b32 s84, s96, 15\n"
    "s_add_u32 s85, s84, 131072\n"
    "s_add_u32 s86, s84, 262144\n"
    "s_add_u32 s87, s84, 393216\n"
    "s_mul_i32 s96, s92, 14\n"
    "s_add_u32 s96, s96, s36\n"
    "s_lshl_b32 s96, s96, 15\n"
    "s_add_u32 s88, s96, 65536\n"
    "s_add_u32 s89, s96, 196608\n"
    "s_add_u32 s90, s96, 327680\n"
    "s_sub_u32 s96, s96, 65536\n"
    "s_cmp_lt_u32 s36, 2\n"
    "s_cselect_b32 s87, s87, s96\n"
    "s_mov_b32 s58, 8192\n"
    "s_mov_b32 s59, 16384\n"
    "s_mov_b32 s60, 24576\n"
    "s_mov_b32 s61, 32768\n"
    "s_mov_b32 s62, 40960\n"
    "s_mov_b32 s63, 49152\n"
    "v_lshrrev_b32 v3, 3, v1\n"
    "v_and_b32 v5, 6, v3\n"
    "v_lshlrev_b32 v3, 10, v3\n"
    "v_lshl_add_u32 v3, s99, 7, v3\n"
    "v_and_b32 v4, 7, v1\n"
    "v_xor_b32 v4, v4, v5\n"
    "v_lshl_add_u32 v151, v4, 4, v3\n"
    "v_add_u32 v152, 7168, v151\n"
    "v_add_u32 v153, 14336, v151\n"
    "v_add_u32 v154, 21504, v151\n"
    "v_lshrrev_b32 v5, 2, v1\n"
    "v_lshlrev_b32 v5, 7, v5\n"
    "v_bfe_u32 v3, %2, 6, 1\n"
    "v_lshl_add_u32 v5, v3, 12, v5\n"
    "v_and_b32 v6, 3, %2\n"
    "v_lshl_add_u32 v5, v6, 3, v5\n"
    "v_bfe_u32 v4, %2, 3, 2\n"
    "v_lshl_add_u32 v163, v4, 5, v5\n"
    "v_xor_b32 v166, 32, v163\n"
    "v_xor_b32 v169, 64, v163\n"
    "v_xor_b32 v172, 96, v163\n"
    "v_add_u32 v164, 0x10000, v163\n"
    "v_add_u32 v165, 0x20000, v163\n"
    "v_add_u32 v167, 0x10000, v166\n"
    "v_add_u32 v168, 0x20000, v166\n"
    "v_add_u32 v170, 0x10000, v169\n"
    "v_add_u32 v171, 0x20000, v169\n"
    "v_add_u32 v173, 0x10000, v172\n"
    "v_add_u32 v174, 0x20000, v172\n"
    "s_lshl_b32 s93, s33, 2\n"
    "s_add_u32 s93, s93, s65\n"
    "s_lshl_b32 s96, s33, 16\n"
    "s_lshl_b32 s97, s65, 7\n"
    "s_add_u32 s96, s96, s97\n"
    "s_and_b32 s97, s36, 1\n"
    "s_lshl_b32 s97, s97, 9\n"
    "s_add_u32 s96, s96, s97\n"
    "s_add_u32 s96, s96, 65536\n"
    "v_min_u32 v2, 13, v1\n"
    "v_lshl_add_u32 v2, v2, 10, s96\n"
    "s_mul_i32 s96, s65, 14336\n"
    "s_mul_i32 s97, s92, 14336\n"
    "v_add_u32 v157, s96, v2\n"
    "v_add_u32 v158, s97, v2\n"
    "v_xor_b32 v159, 0x100, v157\n"
    "v_xor_b32 v160, 0x100, v158\n"
    "s_sub_u32 s42, s92, s65\n"
    "s_mul_i32 s42, s42, 57344\n"
    "s_xor_b32 s92, s93, 2\n"
    "s_sub_u32 s54, s92, s93\n"
    "s_mul_i32 s54, s54, 229376\n"
    "s_sub_u32 s54, s54, s42\n"
    "s_mov_b32 s98, s36\n"
    "s_lshl_b32 s96, s93, 2\n"
    "s_add_u32 s96, s96, s65\n"
    "s_mul_i32 s96, s96, 14\n"
    "s_and_b32 s97, s36, 1\n"
    "s_add_u32 s96, s96, s97\n"
    "s_lshl_b32 s96, s96, 2\n"
    "s_add_u32 s96, s96, s98\n"
    "s_lshl_b32 s96, s96, 10\n"
    "s_add_u32 s96, s96, 1048576\n"
    "v_lshlrev_b32 v136, 4, v1\n"
    "v_add_u32 v162, s96, v136\n"
    "v_xor_b32 v161, 0x400, v162\n"
    "s_mov_b32 s79, s98\n"
    "v_lshrrev_b32 v3, 5, %2\n"
    "v_min_u32 v3, 1, v3\n"
    "v_lshlrev_b32 v3, 31, v3\n"
    "v_lshl_or_b32 v156, %2, 2, v3\n"
    "s_lshr_b32 s99, s38, 2\n"
    "s_xor_b32 s99, s99, 3\n"
    "s_lshl_b32 s93, s33, 2\n"
    "s_add_u32 s93, s93, s65\n"
    "s_xor_b32 s92, s93, 2\n"
    "v_lshl_add_u32 v175, s36, 12, v136\n"
    "v_xor_b32 v176, 0x1000, v175\n"
    "s_lshl_b32 s96, s93, 3\n"
    "s_add_u32 s96, s96, s99\n"
    "s_lshl_b32 s96, s96, 2\n"
    "s_add_u32 s96, s96, s79\n"
    "v_lshl_add_u32 v136, s96, 10, v136\n"
    "v_xor_b32 v253, 65536, v136\n"
    "v_lshrrev_b32 v255, 2, v136\n"
    "v_lshrrev_b32 v254, 2, v253\n"
    "s_mov_b32 s40, 9437184\n"
    "s_mov_b32 s41, 10485760\n"
    "s_mov_b32 s72, 11534336\n"
    "s_mov_b32 s43, 0xffff0000\n"
    "s_lshl_b32 s96, s92, 16\n"
    "s_lshl_b32 s97, s79, 14\n"
    "s_add_u32 s96, s96, s97\n"
    "s_lshl_b32 s97, s99, 7\n"
    "s_add_u32 s55, s96, s97\n"
    "v_and_b32 v3, 15, v1\n"
    "v_lshlrev_b32 v3, 10, v3\n"
    "v_bfe_u32 v4, v1, 4, 1\n"
    "v_lshl_add_u32 v3, v4, 5, v3\n"
    "v_lshrrev_b32 v4, 5, v1\n"
    "v_lshl_add_u32 v177, v4, 4, v3\n"
    "s_waitcnt lgkmcnt(0)\n"
    "s_add_u32 s56, s56, 1\n"
    "s_addc_u32 s57, s57, 0\n"
    "s_and_b32 s39, s56, 1\n"
    "s_add_u32 s39, s39, 2\n"
    "s_lshl_b32 s39, s39, 24\n"
    "s_add_u32 s20, s20, s39\n"
    "s_addc_u32 s21, s21, 0\n"
    "v_mov_b32 v132, s56\n"
    "v_mov_b32 v3, s57\n"
    "v_and_b32 v4, 1, %2\n"
    "v_cmp_eq_u32 vcc, 1, v4\n"
    "s_lshl_b32 s35, %1, 7\n"
    "s_add_u32 s35, s35, 32768\n"
    "v_cndmask_b32 v133, v132, v3, vcc\n"
    "s_barrier\n"
    "buffer_store_dword v133, v156, s[20:23], s35 offen sc1\n"
    "v_lshlrev_b32 v2, 7, v1\n"
    "s_mul_i32 s99, s65, 56\n"
    "s_mul_i32 s92, s45, 7\n"
    "s_add_u32 s99, s99, s92\n"
    "s_add_u32 s99, s99, s44\n"
    "s_lshl_b32 s99, s99, 13\n"
    "buffer_load_dword v16, v2, s[12:15], s99 offen\n"
    "s_add_u32 s99, s99, 131072\n"
    "s_cmp_lt_u32 s44, 12\n"
    "s_cselect_b32 s99, s99, 0x7f000000\n"
    "buffer_load_dword v17, v2, s[12:15], s99 offen\n"
    "s_add_u32 m0, s64, 32768\n"
    "s_nop 0\n"
    "buffer_load_dwordx4 v151, s[12:15], s84 offen lds\n"
    "buffer_load_dwordx4 v152, s[12:15], s84 offen offset:1024 lds\n"
    "buffer_load_dwordx4 v153, s[12:15], s84 offen offset:2048 lds\n"
    "buffer_load_dwordx4 v154, s[12:15], s84 offen offset:3072 lds\n"
    "s_add_u32 m0, s64, 49152\n"
    "s_nop 0\n"
    "buffer_load_dwordx4 v151, s[12:15], s85 offen lds\n"
    "buffer_load_dwordx4 v152, s[12:15], s85 offen offset:1024 lds\n"
    "buffer_load_dwordx4 v153, s[12:15], s85 offen offset:2048 lds\n"
    "buffer_load_dwordx4 v154, s[12:15], s85 offen offset:3072 lds\n"
    "s_add_u32 m0, s64, 65536\n"
    "s_nop 0\n"
    "buffer_load_dwordx4 v151, s[12:15], s86 offen lds\n"
    "buffer_load_dwordx4 v152, s[12:15], s86 offen offset:1024 lds\n"
    "buffer_load_dwordx4 v153, s[12:15], s86 offen offset:2048 lds\n"
    "buffer_load_dwordx4 v154, s[12:15], s86 offen offset:3072 lds\n"
    "s_add_u32 m0, s64, 81920\n"
    "s_nop 0\n"
    "buffer_load_dwordx4 v151, s[12:15], s87 offen lds\n"
    "buffer_load_dwordx4 v152, s[12:15], s87 offen offset:1024 lds\n"
    "buffer_load_dwordx4 v153, s[12:15], s87 offen offset:2048 lds\n"
    "buffer_load_dwordx4 v154, s[12:15], s87 offen offset:3072 lds\n"
    "s_add_u32 m0, s64, 131072\n"
    "s_nop 0\n"
    "buffer_load_dwordx4 v151, s[12:15], s88 offen lds\n"
    "buffer_load_dwordx4 v152, s[12:15], s88 offen offset:1024 lds\n"
    "buffer_load_dwordx4 v153, s[12:15], s88 offen offset:2048 lds\n"
    "buffer_load_dwordx4 v154, s[12:15], s88 offen offset:3072 lds\n"
    "s_add_u32 m0, s64, 147456\n"
    "s_nop 0\n"
    "buffer_load_dwordx4 v151, s[12:15], s89 offen lds\n"
    "buffer_load_dwordx4 v152, s[12:15], s89 offen offset:1024 lds\n"
    "buffer_load_dwordx4 v153, s[12:15], s89 offen offset:2048 lds\n"
    "buffer_load_dwordx4 v154, s[12:15], s89 offen offset:3072 lds\n"
    "s_add_u32 m0, s64, 0\n"
    "s_nop 0\n"
    "buffer_load_dwordx4 v151, s[12:15], s90 offen lds\n"
    "buffer_load_dwordx4 v152, s[12:15], s90 offen offset:1024 lds\n"
    "buffer_load_dwordx4 v153, s[12:15], s90 offen offset:2048 lds\n"
    "buffer_load_dwordx4 v154, s[12:15], s90 offen offset:3072 lds\n"
    "s_or_b32 s96, %1, s36\n"
    "s_cmp_lg_u32 s96, 0\n"
    "s_cbranch_scc1 L_nobump\n"
    "v_lshlrev_b32 v2, 9, v1\n"
    "s_mov_b32 s96, 0\n"
    "s_mov_b32 s97, 32768\n"
    "L_bump:\n"
    "buffer_load_dwordx2 v[32:33], v2, s[20:23], s97 offen offset:0 sc1\n"
    "buffer_load_dwordx2 v[34:35], v2, s[20:23], s97 offen offset:128 sc1\n"
    "buffer_load_dwordx2 v[36:37], v2, s[20:23], s97 offen offset:256 sc1\n"
    "buffer_load_dwordx2 v[38:39], v2, s[20:23], s97 offen offset:384 sc1\n"
    "s_waitcnt vmcnt(0)\n"
    "v_cmp_ne_u64 vcc, s[56:57], v[32:33]\n"
    "v_cmp_ne_u64 s[98:99], s[56:57], v[34:35]\n"
    "v_cmp_ne_u64 s[92:93], s[56:57], v[36:37]\n"
    "s_or_b64 s[98:99], s[98:99], vcc\n"
    "v_cmp_ne_u64 vcc, s[56:57], v[38:39]\n"
    "s_or_b64 s[98:99], s[98:99], s[92:93]\n"
    "s_or_b64 s[98:99], s[98:99], vcc\n"
    "s_cbranch_scc0 L_dobump\n"
    "s_sleep 8\n"
    "s_add_u32 s96, s96, 1\n"
    "s_cmp_lt_u32 s96, 65536\n"
    "s_cbranch_scc1 L_bump\n"
    "L_dobump:\n"
    "s_sub_u32 s8, s20, s39\n"
    "s_subb_u32 s9, s21, 0\n"
    "s_mov_b32 s10, 128\n"
    "s_mov_b32 s11, 0x20000\n"
    "buffer_store_dword v133, v156, s[8:11], 0 offen sc1\n"
    "L_nobump:\n"
    "s_waitcnt lgkmcnt(0)\n"
    "L_lw0:\n"
    "s_memtime s[80:81]\n"
    "s_waitcnt lgkmcnt(0)\n"
    "s_sub_u32 s80, s80, s82\n"
    "s_cmp_lt_u32 s80, 11000\n"
    "s_cbranch_scc1 L_lw0\n"
    "buffer_load_dwordx2 v[134:135], v157, s[20:23], 0 offen sc1\n"
    "s_waitcnt vmcnt(0)\n"
    "v_cmp_ne_u64 vcc, s[56:57], v[134:135]\n"
    "s_cbranch_vccnz L_spin0i\n"
    "L_spin0i_back:\n"
    "buffer_load_dwordx4 v[188:191], v161, s[20:23], 0 offen sc1\n"
    "buffer_load_dwordx4 v[192:195], v162, s[20:23], 0 offen sc1\n"
    "buffer_load_dwordx4 v[196:199], v161, s[20:23], s58 offen sc1\n"
    "buffer_load_dwordx4 v[200:203], v162, s[20:23], s58 offen sc1\n"
    "buffer_load_dwordx4 v[204:207], v161, s[20:23], s59 offen sc1\n"
    "buffer_load_dwordx4 v[208:211], v162, s[20:23], s59 offen sc1\n"
    "buffer_load_dwordx4 v[212:215], v161, s[20:23], s60 offen sc1\n"
    "buffer_load_dwordx4 v[216:219], v162, s[20:23], s60 offen sc1\n"
    "buffer_load_dwordx4 v[220:223], v161, s[20:23], s61 offen sc1\n"
    "buffer_load_dwordx4 v[224:227], v162, s[20:23], s61 offen sc1\n"
    "s_waitcnt vmcnt(11)\n"
    "s_barrier\n"
    "ds_read_b64_tr_b16 a[32:33], v163 offset:32768\n"
    "ds_read_b64_tr_b16 a[34:35], v163 offset:34816\n"
    "s_waitcnt vmcnt(9) lgkmcnt(0)\n"
    "v_mfma_f32_16x16x32_bf16 v[80:83], a[32:35], v[188:191], 0\n"
    "ds_read_b64_tr_b16 a[36:37], v166 offset:32768\n"
    "ds_read_b64_tr_b16 a[38:39], v166 offset:34816\n"
    "ds_read_b64_tr_b16 a[40:41], v169 offset:32768\n"
    "ds_read_b64_tr_b16 a[42:43], v169 offset:34816\n"
    "ds_read_b64_tr_b16 a[44:45], v172 offset:32768\n"
    "buffer_load_dwordx4 v[228:231], v161, s[20:23], s62 offen sc1\n"
    "ds_read_b64_tr_b16 a[46:47], v172 offset:34816\n"
    "ds_read_b64_tr_b16 a[48:49], v163 offset:40960\n"
    "ds_read_b64_tr_b16 a[50:51], v163 offset:43008\n"
    "ds_read_b64_tr_b16 a[52:53], v166 offset:40960\n"
    "ds_read_b64_tr_b16 a[54:55], v166 offset:43008\n"
    "s_waitcnt vmcnt(9)\n"
    "v_mfma_f32_16x16x32_bf16 v[96:99], a[32:35], v[192:195], 0\n"
    "s_waitcnt lgkmcnt(8)\n"
    "ds_read_b64_tr_b16 a[56:57], v169 offset:40960\n"
    "ds_read_b64_tr_b16 a[58:59], v169 offset:43008\n"
    "v_mfma_f32_16x16x32_bf16 v[84:87], a[36:39], v[188:191], 0\n"
    "buffer_load_dwordx4 v[232:235], v162, s[20:23], s62 offen sc1\n"
    "v_mfma_f32_16x16x32_bf16 v[100:103], a[36:39], v[192:195], 0\n"
    "s_waitcnt lgkmcnt(8)\n"
    "ds_read_b64_tr_b16 a[60:61], v172 offset:40960\n"
    "ds_read_b64_tr_b16 a[62:63], v172 offset:43008\n"
    "v_mfma_f32_16x16x32_bf16 v[88:91], a[40:43], v[188:191], 0\n"
    "ds_read_b64_tr_b16 a[64:65], v163 offset:49152\n"
    "ds_read_b64_tr_b16 a[66:67], v163 offset:51200\n"
    "v_mfma_f32_16x16x32_bf16 v[104:107], a[40:43], v[192:195], 0\n"
    "s_waitcnt lgkmcnt(10)\n"
    "s_waitcnt lgkmcnt(0)\n"
    "L_lw1:\n"
    "s_memtime s[80:81]\n"
    "s_waitcnt lgkmcnt(0)\n"
    "s_sub_u32 s80, s80, s82\n"
    "s_cmp_lt_u32 s80, 12400\n"
    "s_cbranch_scc1 L_lw1\n"
    "buffer_load_dwordx2 v[134:135], v158, s[20:23], 0 offen sc1\n"
    "v_mfma_f32_16x16x32_bf16 v[92:95], a[44:47], v[188:191], 0\n"
    "ds_read_b64_tr_b16 a[68:69], v166 offset:49152\n"
    "ds_read_b64_tr_b16 a[70:71], v166 offset:51200\n"
    "v_mfma_f32_16x16x32_bf16 v[108:111], a[44:47], v[192:195], 0\n"
    "s_waitcnt vmcnt(10)\n"
    "ds_read_b64_tr_b16 a[72:73], v169 offset:49152\n"
    "ds_read_b64_tr_b16 a[74:75], v169 offset:51200\n"
    "v_mfma_f32_16x16x32_bf16 v[80:83], a[48:51], v[196:199], v[80:83]\n"
    "s_waitcnt vmcnt(9)\n"
    "ds_read_b64_tr_b16 a[76:77], v172 offset:49152\n"
    "ds_read_b64_tr_b16 a[78:79], v172 offset:51200\n"
    "v_mfma_f32_16x16x32_bf16 v[96:99], a[48:51], v[200:203], v[96:99]\n"
    "buffer_load_dwordx4 v[236:239], v161, s[20:23], s63 offen sc1\n"
    "v_mfma_f32_16x16x32_bf16 v[84:87], a[52:55], v[196:199], v[84:87]\n"
    "ds_read_b64_tr_b16 a[80:81], v163 offset:57344\n"
    "ds_read_b64_tr_b16 a[82:83], v163 offset:59392\n"
    "v_mfma_f32_16x16x32_bf16 v[100:103], a[52:55], v[200:203], v[100:103]\n"
    "ds_read_b64_tr_b16 a[84:85], v166 offset:57344\n"
    "ds_read_b64_tr_b16 a[86:87], v166 offset:59392\n"
    "v_mfma_f32_16x16x32_bf16 v[88:91], a[56:59], v[196:199], v[88:91]\n"
    "ds_read_b64_tr_b16 a[88:89], v169 offset:57344\n"
    "ds_read_b64_tr_b16 a[90:91], v169 offset:59392\n"
    "v_mfma_f32_16x16x32_bf16 v[104:107], a[56:59], v[200:203], v[104:107]\n"
    "buffer_load_dwordx4 v[240:243], v162, s[20:23], s63 offen sc1\n"
    "v_mfma_f32_16x16x32_bf16 v[92:95], a[60:63], v[196:199], v[92:95]\n"
    "ds_read_b64_tr_b16 a[92:93], v172 offset:57344\n"
    "ds_read_b64_tr_b16 a[94:95], v172 offset:59392\n"
    "v_mfma_f32_16x16x32_bf16 v[108:111], a[60:63], v[200:203], v[108:111]\n"
    "s_waitcnt vmcnt(10)\n"
    "ds_read_b64_tr_b16 a[96:97], v164 offset:0\n"
    "ds_read_b64_tr_b16 a[98:99], v164 offset:2048\n"
    "v_mfma_f32_16x16x32_bf16 v[80:83], a[64:67], v[204:207], v[80:83]\n"
    "s_waitcnt vmcnt(9)\n"
    "ds_read_b64_tr_b16 a[100:101], v167 offset:0\n"
    "ds_read_b64_tr_b16 a[102:103], v167 offset:2048\n"
    "v_mfma_f32_16x16x32_bf16 v[96:99], a[64:67], v[208:211], v[96:99]\n"
    "s_waitcnt lgkmcnt(14)\n"
    "ds_read_b64_tr_b16 a[104:105], v170 offset:0\n"
    "ds_read_b64_tr_b16 a[106:107], v170 offset:2048\n"
    "v_mfma_f32_16x16x32_bf16 v[84:87], a[68:71], v[204:207], v[84:87]\n"
    "ds_read_b64_tr_b16 a[108:109], v173 offset:0\n"
    "ds_read_b64_tr_b16 a[110:111], v173 offset:2048\n"
    "v_mfma_f32_16x16x32_bf16 v[100:103], a[68:71], v[208:211], v[100:103]\n"
    "ds_read_b64_tr_b16 a[112:113], v164 offset:8192\n"
    "ds_read_b64_tr_b16 a[114:115], v164 offset:10240\n"
    "v_mfma_f32_16x16x32_bf16 v[88:91], a[72:75], v[204:207], v[88:91]\n"
    "ds_read_b64_tr_b16 a[116:117], v167 offset:8192\n"
    "ds_read_b64_tr_b16 a[118:119], v167 offset:10240\n"
    "v_mfma_f32_16x16x32_bf16 v[104:107], a[72:75], v[208:211], v[104:107]\n"
    "s_waitcnt lgkmcnt(14)\n"
    "ds_read_b64_tr_b16 a[120:121], v170 offset:8192\n"
    "ds_read_b64_tr_b16 a[122:123], v170 offset:10240\n"
    "v_mfma_f32_16x16x32_bf16 v[92:95], a[76:79], v[204:207], v[92:95]\n"
    "ds_read_b64_tr_b16 a[124:125], v173 offset:8192\n"
    "ds_read_b64_tr_b16 a[126:127], v173 offset:10240\n"
    "v_mfma_f32_16x16x32_bf16 v[108:111], a[76:79], v[208:211], v[108:111]\n"
    "s_waitcnt vmcnt(8)\n"
    "ds_read_b64_tr_b16 a[128:129], v164 offset:16384\n"
    "ds_read_b64_tr_b16 a[130:131], v164 offset:18432\n"
    "v_mfma_f32_16x16x32_bf16 v[80:83], a[80:83], v[212:215], v[80:83]\n"
    "s_waitcnt vmcnt(7)\n"
    "ds_read_b64_tr_b16 a[132:133], v167 offset:16384\n"
    "ds_read_b64_tr_b16 a[134:135], v167 offset:18432\n"
    "v_mfma_f32_16x16x32_bf16 v[96:99], a[80:83], v[216:219], v[96:99]\n"
    "ds_read_b64_tr_b16 a[136:137], v170 offset:16384\n"
    "ds_read_b64_tr_b16 a[138:139], v170 offset:18432\n"
    "v_mfma_f32_16x16x32_bf16 v[84:87], a[84:87], v[212:215], v[84:87]\n"
    "ds_read_b64_tr_b16 a[140:141], v173 offset:16384\n"
    "ds_read_b64_tr_b16 a[142:143], v173 offset:18432\n"
    "v_mfma_f32_16x16x32_bf16 v[100:103], a[84:87], v[216:219], v[100:103]\n"
    "ds_read_b64_tr_b16 a[144:145], v164 offset:24576\n"
    "ds_read_b64_tr_b16 a[146:147], v164 offset:26624\n"
    "v_mfma_f32_16x16x32_bf16 v[88:91], a[88:91], v[212:215], v[88:91]\n"
    "ds_read_b64_tr_b16 a[160:161], v165 offset:0\n"
    "ds_read_b64_tr_b16 a[162:163], v165 offset:2048\n"
    "v_mfma_f32_16x16x32_bf16 v[104:107], a[88:91], v[216:219], v[104:107]\n"
    "s_waitcnt lgkmcnt(14)\n"
    "ds_read_b64_tr_b16 a[176:177], v165 offset:8192\n"
    "ds_read_b64_tr_b16 a[178:179], v165 offset:10240\n"
    "v_mfma_f32_16x16x32_bf16 v[92:95], a[92:95], v[212:215], v[92:95]\n"
    "ds_read_b64_tr_b16 a[192:193], v165 offset:16384\n"
    "ds_read_b64_tr_b16 a[194:195], v165 offset:18432\n"
    "v_mfma_f32_16x16x32_bf16 v[108:111], a[92:95], v[216:219], v[108:111]\n"
    "s_waitcnt vmcnt(6)\n"
    "ds_read_b64_tr_b16 a[208:209], v165 offset:24576\n"
    "ds_read_b64_tr_b16 a[210:211], v165 offset:26624\n"
    "v_mfma_f32_16x16x32_bf16 v[80:83], a[96:99], v[220:223], v[80:83]\n"
    "s_waitcnt vmcnt(5)\n"
    "ds_read_b64_tr_b16 a[224:225], v163 offset:0\n"
    "ds_read_b64_tr_b16 a[226:227], v163 offset:2048\n"
    "v_mfma_f32_16x16x32_bf16 v[96:99], a[96:99], v[224:227], v[96:99]\n"
    "ds_read_b64_tr_b16 a[240:241], v163 offset:8192\n"
    "ds_read_b64_tr_b16 a[242:243], v163 offset:10240\n"
    "v_mfma_f32_16x16x32_bf16 v[84:87], a[100:103], v[220:223], v[84:87]\n"
    "ds_read_b64_tr_b16 a[148:149], v167 offset:24576\n"
    "ds_read_b64_tr_b16 a[150:151], v167 offset:26624\n"
    "v_mfma_f32_16x16x32_bf16 v[100:103], a[100:103], v[224:227], v[100:103]\n"
    "ds_read_b64_tr_b16 a[164:165], v168 offset:0\n"
    "ds_read_b64_tr_b16 a[166:167], v168 offset:2048\n"
    "v_mfma_f32_16x16x32_bf16 v[88:91], a[104:107], v[220:223], v[88:91]\n"
    "ds_read_b64_tr_b16 a[180:181], v168 offset:8192\n"
    "ds_read_b64_tr_b16 a[182:183], v168 offset:10240\n"
    "v_mfma_f32_16x16x32_bf16 v[104:107], a[104:107], v[224:227], v[104:107]\n"
    "ds_read_b64_tr_b16 a[196:197], v168 offset:16384\n"
    "ds_read_b64_tr_b16 a[198:199], v168 offset:18432\n"
    "v_mfma_f32_16x16x32_bf16 v[92:95], a[108:111], v[220:223], v[92:95]\n"
    "ds_read_b64_tr_b16 a[212:213], v168 offset:24576\n"
    "ds_read_b64_tr_b16 a[214:215], v168 offset:26624\n"
    "v_mfma_f32_16x16x32_bf16 v[108:111], a[108:111], v[224:227], v[108:111]\n"
    "s_waitcnt vmcnt(4)\n"
    "ds_read_b64_tr_b16 a[228:229], v166 offset:0\n"
    "ds_read_b64_tr_b16 a[230:231], v166 offset:2048\n"
    "v_mfma_f32_16x16x32_bf16 v[80:83], a[112:115], v[228:231], v[80:83]\n"
    "s_waitcnt vmcnt(3)\n"
    "s_waitcnt vmcnt(2)\n"
    "v_cmp_ne_u64 vcc, s[56:57], v[134:135]\n"
    "s_cbranch_vccnz L_spin1i\n"
    "L_spin1i_back:\n"
    "v_add_u32 v161, s42, v161\n"
    "v_add_u32 v162, s42, v162\n"
    "v_mfma_f32_16x16x32_bf16 v[96:99], a[112:115], v[232:235], v[96:99]\n"
    "buffer_load_dwordx4 v[48:51], v161, s[20:23], 0 offen sc1\n"
    "v_mfma_f32_16x16x32_bf16 v[84:87], a[116:119], v[228:231], v[84:87]\n"
    "ds_read_b64_tr_b16 a[244:245], v166 offset:8192\n"
    "ds_read_b64_tr_b16 a[246:247], v166 offset:10240\n"
    "v_mfma_f32_16x16x32_bf16 v[100:103], a[116:119], v[232:235], v[100:103]\n"
    "ds_read_b64_tr_b16 a[152:153], v170 offset:24576\n"
    "ds_read_b64_tr_b16 a[154:155], v170 offset:26624\n"
    "v_mfma_f32_16x16x32_bf16 v[88:91], a[120:123], v[228:231], v[88:91]\n"
    "ds_read_b64_tr_b16 a[168:169], v171 offset:0\n"
    "ds_read_b64_tr_b16 a[170:171], v171 offset:2048\n"
    "v_mfma_f32_16x16x32_bf16 v[104:107], a[120:123], v[232:235], v[104:107]\n"
    "s_waitcnt lgkmcnt(14)\n"
    "buffer_load_dwordx4 v[52:55], v161, s[20:23], s58 offen sc1\n"
    "v_mfma_f32_16x16x32_bf16 v[92:95], a[124:127], v[228:231], v[92:95]\n"
    "ds_read_b64_tr_b16 a[184:185], v171 offset:8192\n"
    "ds_read_b64_tr_b16 a[186:187], v171 offset:10240\n"
    "v_mfma_f32_16x16x32_bf16 v[108:111], a[124:127], v[232:235], v[108:111]\n"
    "s_waitcnt vmcnt(3)\n"
    "ds_read_b64_tr_b16 a[200:201], v171 offset:16384\n"
    "ds_read_b64_tr_b16 a[202:203], v171 offset:18432\n"
    "v_mfma_f32_16x16x32_bf16 v[80:83], a[128:131], v[236:239], v[80:83]\n"
    "s_waitcnt vmcnt(2)\n"
    "ds_read_b64_tr_b16 a[216:217], v171 offset:24576\n"
    "ds_read_b64_tr_b16 a[218:219], v171 offset:26624\n"
    "v_mfma_f32_16x16x32_bf16 v[96:99], a[128:131], v[240:243], v[96:99]\n"
    "buffer_load_dwordx4 v[56:59], v161, s[20:23], s59 offen sc1\n"
    "v_mfma_f32_16x16x32_bf16 v[84:87], a[132:135], v[236:239], v[84:87]\n"
    "ds_read_b64_tr_b16 a[232:233], v169 offset:0\n"
    "ds_read_b64_tr_b16 a[234:235], v169 offset:2048\n"
    "v_mfma_f32_16x16x32_bf16 v[100:103], a[132:135], v[240:243], v[100:103]\n"
    "ds_read_b64_tr_b16 a[248:249], v169 offset:8192\n"
    "ds_read_b64_tr_b16 a[250:251], v169 offset:10240\n"
    "v_mfma_f32_16x16x32_bf16 v[88:91], a[136:139], v[236:239], v[88:91]\n"
    "ds_read_b64_tr_b16 a[156:157], v173 offset:24576\n"
    "ds_read_b64_tr_b16 a[158:159], v173 offset:26624\n"
    "v_mfma_f32_16x16x32_bf16 v[104:107], a[136:139], v[240:243], v[104:107]\n"
    "buffer_load_dwordx4 v[60:63], v161, s[20:23], s60 offen sc1\n"
    "v_mfma_f32_16x16x32_bf16 v[92:95], a[140:143], v[236:239], v[92:95]\n"
    "ds_read_b64_tr_b16 a[172:173], v174 offset:0\n"
    "ds_read_b64_tr_b16 a[174:175], v174 offset:2048\n"
    "v_mfma_f32_16x16x32_bf16 v[108:111], a[140:143], v[240:243], v[108:111]\n"
    "ds_read_b64_tr_b16 a[188:189], v174 offset:8192\n"
    "ds_read_b64_tr_b16 a[190:191], v174 offset:10240\n"
    "ds_read_b64_tr_b16 a[204:205], v174 offset:16384\n"
    "ds_read_b64_tr_b16 a[206:207], v174 offset:18432\n"
    "ds_read_b64_tr_b16 a[220:221], v174 offset:24576\n"
    "ds_read_b64_tr_b16 a[222:223], v174 offset:26624\n"
    "ds_read_b64_tr_b16 a[236:237], v172 offset:0\n"
    "ds_read_b64_tr_b16 a[238:239], v172 offset:2048\n"
    "buffer_load_dwordx4 v[64:67], v161, s[20:23], s61 offen sc1\n"
    "ds_read_b64_tr_b16 a[252:253], v172 offset:8192\n"
    "ds_read_b64_tr_b16 a[254:255], v172 offset:10240\n"
    "buffer_load_dwordx4 v[68:71], v161, s[20:23], s62 offen sc1\n"
    "buffer_load_dwordx4 v[72:75], v161, s[20:23], s63 offen sc1\n"
    "buffer_load_dwordx4 v[76:79], v162, s[20:23], 0 offen sc1\n"
    "buffer_load_dwordx4 a[0:3], v162, s[20:23], s58 offen sc1\n"
    "s_waitcnt vmcnt(8)\n"
    "v_mfma_f32_16x16x32_bf16 v[80:83], a[144:147], v[48:51], v[80:83]\n"
    "buffer_load_dwordx4 a[4:7], v162, s[20:23], s59 offen sc1\n"
    "s_waitcnt lgkmcnt(0)\n"
    "L_lw2:\n"
    "s_memtime s[80:81]\n"
    "s_waitcnt lgkmcnt(0)\n"
    "s_sub_u32 s80, s80, s82\n"
    "s_cmp_lt_u32 s80, 13800\n"
    "s_cbranch_scc1 L_lw2\n"
    "buffer_load_dwordx2 v[134:135], v159, s[20:23], 0 offen sc1\n"
    "s_waitcnt vmcnt(9)\n"
    "v_mfma_f32_16x16x32_bf16 v[80:83], a[160:163], v[52:55], v[80:83]\n"
    "buffer_load_dwordx4 a[8:11], v162, s[20:23], s60 offen sc1\n"
    "s_waitcnt vmcnt(9)\n"
    "v_mfma_f32_16x16x32_bf16 v[80:83], a[176:179], v[56:59], v[80:83]\n"
    "buffer_load_dwordx4 a[12:15], v162, s[20:23], s61 offen sc1\n"
    "s_waitcnt vmcnt(9)\n"
    "v_mfma_f32_16x16x32_bf16 v[80:83], a[192:195], v[60:63], v[80:83]\n"
    "buffer_load_dwordx4 a[16:19], v162, s[20:23], s62 offen sc1\n"
    "s_waitcnt vmcnt(9)\n"
    "v_mfma_f32_16x16x32_bf16 v[80:83], a[208:211], v[64:67], v[80:83]\n"
    "buffer_load_dwordx4 a[20:23], v162, s[20:23], s63 offen sc1\n"
    "s_waitcnt vmcnt(9)\n"
    "v_mfma_f32_16x16x32_bf16 v[80:83], a[224:227], v[68:71], v[80:83]\n"
    "s_waitcnt vmcnt(8)\n"
    "v_mfma_f32_16x16x32_bf16 v[80:83], a[240:243], v[72:75], v[80:83]\n"
    "v_mfma_f32_16x16x32_bf16 v[84:87], a[148:151], v[48:51], v[84:87]\n"
    "v_mfma_f32_16x16x32_bf16 v[84:87], a[164:167], v[52:55], v[84:87]\n"
    "v_mfma_f32_16x16x32_bf16 v[84:87], a[180:183], v[56:59], v[84:87]\n"
    "v_mfma_f32_16x16x32_bf16 v[84:87], a[196:199], v[60:63], v[84:87]\n"
    "v_mfma_f32_16x16x32_bf16 v[84:87], a[212:215], v[64:67], v[84:87]\n"
    "v_mfma_f32_16x16x32_bf16 v[84:87], a[228:231], v[68:71], v[84:87]\n"
    "v_mfma_f32_16x16x32_bf16 v[84:87], a[244:247], v[72:75], v[84:87]\n"
    "v_mfma_f32_16x16x32_bf16 v[88:91], a[152:155], v[48:51], v[88:91]\n"
    "ds_write_b128 v175, v[80:83] offset:16384\n"
    "v_mfma_f32_16x16x32_bf16 v[88:91], a[168:171], v[52:55], v[88:91]\n"
    "v_mfma_f32_16x16x32_bf16 v[88:91], a[184:187], v[56:59], v[88:91]\n"
    "v_mfma_f32_16x16x32_bf16 v[88:91], a[200:203], v[60:63], v[88:91]\n"
    "v_mfma_f32_16x16x32_bf16 v[88:91], a[216:219], v[64:67], v[88:91]\n"
    "v_mfma_f32_16x16x32_bf16 v[88:91], a[232:235], v[68:71], v[88:91]\n"
    "v_mfma_f32_16x16x32_bf16 v[88:91], a[248:251], v[72:75], v[88:91]\n"
    "v_mfma_f32_16x16x32_bf16 v[92:95], a[156:159], v[48:51], v[92:95]\n"
    "ds_write_b128 v175, v[84:87] offset:17408\n"
    "v_mfma_f32_16x16x32_bf16 v[92:95], a[172:175], v[52:55], v[92:95]\n"
    "s_waitcnt vmcnt(4)\n"
    "v_cmp_ne_u64 vcc, s[56:57], v[134:135]\n"
    "s_cbranch_vccnz L_spin2i\n"
    "L_spin2i_back:\n"
    "v_add_u32 v161, s54, v161\n"
    "v_add_u32 v162, s54, v162\n"
    "v_mfma_f32_16x16x32_bf16 v[92:95], a[188:191], v[56:59], v[92:95]\n"
    "buffer_load_dwordx4 v[112:115], v161, s[20:23], 0 offen sc1\n"
    "v_mfma_f32_16x16x32_bf16 v[92:95], a[204:207], v[60:63], v[92:95]\n"
    "v_mfma_f32_16x16x32_bf16 v[92:95], a[220:223], v[64:67], v[92:95]\n"
    "v_mfma_f32_16x16x32_bf16 v[92:95], a[236:239], v[68:71], v[92:95]\n"
    "v_mfma_f32_16x16x32_bf16 v[92:95], a[252:255], v[72:75], v[92:95]\n"
    "buffer_load_dwordx4 v[116:119], v161, s[20:23], s58 offen sc1\n"
    "v_mfma_f32_16x16x32_bf16 v[96:99], a[144:147], v[76:79], v[96:99]\n"
    "ds_write_b128 v175, v[88:91] offset:18432\n"
    "v_mfma_f32_16x16x32_bf16 v[96:99], a[160:163], a[0:3], v[96:99]\n"
    "v_mfma_f32_16x16x32_bf16 v[96:99], a[176:179], a[4:7], v[96:99]\n"
    "s_waitcnt vmcnt(5)\n"
    "v_mfma_f32_16x16x32_bf16 v[96:99], a[192:195], a[8:11], v[96:99]\n"
    "s_waitcnt vmcnt(4)\n"
    "buffer_load_dwordx4 v[120:123], v161, s[20:23], s59 offen sc1\n"
    "v_mfma_f32_16x16x32_bf16 v[96:99], a[208:211], a[12:15], v[96:99]\n"
    "s_waitcnt vmcnt(4)\n"
    "v_mfma_f32_16x16x32_bf16 v[96:99], a[224:227], a[16:19], v[96:99]\n"
    "s_waitcnt vmcnt(3)\n"
    "v_mfma_f32_16x16x32_bf16 v[96:99], a[240:243], a[20:23], v[96:99]\n"
    "buffer_load_dwordx4 v[124:127], v161, s[20:23], s60 offen sc1\n"
    "v_mfma_f32_16x16x32_bf16 v[100:103], a[148:151], v[76:79], v[100:103]\n"
    "ds_write_b128 v175, v[92:95] offset:19456\n"
    "v_mfma_f32_16x16x32_bf16 v[100:103], a[164:167], a[0:3], v[100:103]\n"
    "v_mfma_f32_16x16x32_bf16 v[100:103], a[180:183], a[4:7], v[100:103]\n"
    "v_mfma_f32_16x16x32_bf16 v[100:103], a[196:199], a[8:11], v[100:103]\n"
    "buffer_load_dwordx4 v[128:131], v161, s[20:23], s61 offen sc1\n"
    "v_mfma_f32_16x16x32_bf16 v[100:103], a[212:215], a[12:15], v[100:103]\n"
    "s_waitcnt lgkmcnt(0)\n"
    "v_mfma_f32_16x16x32_bf16 v[100:103], a[228:231], a[16:19], v[100:103]\n"
    "s_barrier\n"
    "ds_read_b128 v[16:19], v176 offset:16384\n"
    "ds_read_b128 v[20:23], v176 offset:17408\n"
    "v_mfma_f32_16x16x32_bf16 v[100:103], a[244:247], a[20:23], v[100:103]\n"
    "ds_read_b128 v[24:27], v176 offset:18432\n"
    "ds_read_b128 v[28:31], v176 offset:19456\n"
    "v_mfma_f32_16x16x32_bf16 v[104:107], a[152:155], v[76:79], v[104:107]\n"
    "buffer_load_dwordx4 v[138:141], v161, s[20:23], s62 offen sc1\n"
    "v_mfma_f32_16x16x32_bf16 v[104:107], a[168:171], a[0:3], v[104:107]\n"
    "v_mfma_f32_16x16x32_bf16 v[104:107], a[184:187], a[4:7], v[104:107]\n"
    "v_mfma_f32_16x16x32_bf16 v[104:107], a[200:203], a[8:11], v[104:107]\n"
    "v_mfma_f32_16x16x32_bf16 v[104:107], a[216:219], a[12:15], v[104:107]\n"
    "buffer_load_dwordx4 v[142:145], v161, s[20:23], s63 offen sc1\n"
    "v_mfma_f32_16x16x32_bf16 v[104:107], a[232:235], a[16:19], v[104:107]\n"
    "s_waitcnt lgkmcnt(0)\n"
    "v_add_f32 v16, v16, v96\n"
    "v_mfma_f32_16x16x32_bf16 v[104:107], a[248:251], a[20:23], v[104:107]\n"
    "v_add_f32 v17, v17, v97\n"
    "v_add_f32 v18, v18, v98\n"
    "v_mfma_f32_16x16x32_bf16 v[108:111], a[156:159], v[76:79], v[108:111]\n"
    "v_add_f32 v19, v19, v99\n"
    "v_cvt_pk_bf16_f32 v16, v16, v17\n"
    "v_mfma_f32_16x16x32_bf16 v[108:111], a[172:175], a[0:3], v[108:111]\n"
    "buffer_load_dwordx4 v[146:149], v162, s[20:23], 0 offen sc1\n"
    "v_mfma_f32_16x16x32_bf16 v[108:111], a[188:191], a[4:7], v[108:111]\n"
    "v_cvt_pk_bf16_f32 v17, v18, v19\n"
    "v_add3_u32 v15, v132, v16, v17\n"
    "v_mfma_f32_16x16x32_bf16 v[108:111], a[204:207], a[8:11], v[108:111]\n"
    "v_add_f32 v20, v20, v100\n"
    "v_add_f32 v21, v21, v101\n"
    "v_mfma_f32_16x16x32_bf16 v[108:111], a[220:223], a[12:15], v[108:111]\n"
    "v_add_f32 v22, v22, v102\n"
    "v_add_f32 v23, v23, v103\n"
    "v_mfma_f32_16x16x32_bf16 v[108:111], a[236:239], a[16:19], v[108:111]\n"
    "buffer_load_dwordx4 v[178:181], v162, s[20:23], s58 offen sc1\n"
    "v_mfma_f32_16x16x32_bf16 v[108:111], a[252:255], a[20:23], v[108:111]\n"
    "s_waitcnt vmcnt(8)\n"
    "v_cvt_pk_bf16_f32 v18, v20, v21\n"
    "v_cvt_pk_bf16_f32 v19, v22, v23\n"
    "v_mfma_f32_16x16x32_bf16 v[80:83], a[32:35], v[112:115], 0\n"
    "v_add3_u32 v15, v15, v18, v19\n"
    "buffer_load_dwordx4 v[182:185], v162, s[20:23], s59 offen sc1\n"
    "s_waitcnt vmcnt(8)\n"
    "v_mfma_f32_16x16x32_bf16 v[80:83], a[48:51], v[116:119], v[80:83]\n"
    "buffer_store_dwordx4 v[16:19], v136, s[20:23], s40 offen sc1\n"
    "s_waitcnt vmcnt(8)\n"
    "v_mfma_f32_16x16x32_bf16 v[80:83], a[64:67], v[120:123], v[80:83]\n"
    "buffer_load_dwordx4 v[244:247], v162, s[20:23], s60 offen sc1\n"
    "buffer_load_dwordx4 v[248:251], v162, s[20:23], s61 offen sc1\n"
    "s_waitcnt vmcnt(9)\n"
    "v_mfma_f32_16x16x32_bf16 v[80:83], a[80:83], v[124:127], v[80:83]\n"
    "v_add_f32 v24, v24, v104\n"
    "v_add_f32 v25, v25, v105\n"
    "buffer_load_dwordx4 a[24:27], v162, s[20:23], s62 offen sc1\n"
    "s_waitcnt vmcnt(9)\n"
    "v_mfma_f32_16x16x32_bf16 v[80:83], a[96:99], v[128:131], v[80:83]\n"
    "v_add_f32 v26, v26, v106\n"
    "v_add_f32 v27, v27, v107\n"
    "buffer_load_dwordx4 a[28:31], v162, s[20:23], s63 offen sc1\n"
    "s_waitcnt vmcnt(9)\n"
    "v_mfma_f32_16x16x32_bf16 v[80:83], a[112:115], v[138:141], v[80:83]\n"
    "v_cvt_pk_bf16_f32 v20, v24, v25\n"
    "v_cvt_pk_bf16_f32 v21, v26, v27\n"
    "s_waitcnt vmcnt(8)\n"
    "v_mfma_f32_16x16x32_bf16 v[80:83], a[128:131], v[142:145], v[80:83]\n"
    "v_add3_u32 v15, v15, v20, v21\n"
    "v_add_f32 v28, v28, v108\n"
    "v_mfma_f32_16x16x32_bf16 v[84:87], a[36:39], v[112:115], 0\n"
    "v_add_f32 v29, v29, v109\n"
    "v_add_f32 v30, v30, v110\n"
    "v_mfma_f32_16x16x32_bf16 v[84:87], a[52:55], v[116:119], v[84:87]\n"
    "v_add_f32 v31, v31, v111\n"
    "v_cvt_pk_bf16_f32 v22, v28, v29\n"
    "v_mfma_f32_16x16x32_bf16 v[84:87], a[68:71], v[120:123], v[84:87]\n"
    "v_cvt_pk_bf16_f32 v23, v30, v31\n"
    "v_add3_u32 v15, v15, v22, v23\n"
    "v_mfma_f32_16x16x32_bf16 v[84:87], a[84:87], v[124:127], v[84:87]\n"
    "buffer_store_dwordx4 v[20:23], v136, s[20:23], s41 offen sc1\n"
    "v_mfma_f32_16x16x32_bf16 v[84:87], a[100:103], v[128:131], v[84:87]\n"
    "buffer_store_dword v15, v255, s[20:23], s72 offen sc1\n"
    "v_mfma_f32_16x16x32_bf16 v[84:87], a[116:119], v[138:141], v[84:87]\n"
    "v_add_u32 v175, 0x10000, v175\n"
    "v_add_u32 v176, 0x10000, v176\n"
    "v_mfma_f32_16x16x32_bf16 v[84:87], a[132:135], v[142:145], v[84:87]\n"
    "v_mfma_f32_16x16x32_bf16 v[88:91], a[40:43], v[112:115], 0\n"
    "v_mfma_f32_16x16x32_bf16 v[88:91], a[56:59], v[116:119], v[88:91]\n"
    "v_mfma_f32_16x16x32_bf16 v[88:91], a[72:75], v[120:123], v[88:91]\n"
    "v_mfma_f32_16x16x32_bf16 v[88:91], a[88:91], v[124:127], v[88:91]\n"
    "v_mfma_f32_16x16x32_bf16 v[88:91], a[104:107], v[128:131], v[88:91]\n"
    "v_mfma_f32_16x16x32_bf16 v[88:91], a[120:123], v[138:141], v[88:91]\n"
    "v_mfma_f32_16x16x32_bf16 v[88:91], a[136:139], v[142:145], v[88:91]\n"
    "v_mfma_f32_16x16x32_bf16 v[92:95], a[44:47], v[112:115], 0\n"
    "v_mfma_f32_16x16x32_bf16 v[92:95], a[60:63], v[116:119], v[92:95]\n"
    "v_mfma_f32_16x16x32_bf16 v[92:95], a[76:79], v[120:123], v[92:95]\n"
    "v_mfma_f32_16x16x32_bf16 v[92:95], a[92:95], v[124:127], v[92:95]\n"
    "v_mfma_f32_16x16x32_bf16 v[92:95], a[108:111], v[128:131], v[92:95]\n"
    "v_mfma_f32_16x16x32_bf16 v[92:95], a[124:127], v[138:141], v[92:95]\n"
    "v_mfma_f32_16x16x32_bf16 v[92:95], a[140:143], v[142:145], v[92:95]\n"
    "s_waitcnt vmcnt(9)\n"
    "v_mfma_f32_16x16x32_bf16 v[96:99], a[32:35], v[146:149], 0\n"
    "s_waitcnt vmcnt(8)\n"
    "v_mfma_f32_16x16x32_bf16 v[96:99], a[48:51], v[178:181], v[96:99]\n"
    "s_waitcnt vmcnt(7)\n"
    "v_mfma_f32_16x16x32_bf16 v[96:99], a[64:67], v[182:185], v[96:99]\n"
    "s_waitcnt lgkmcnt(0)\n"
    "L_lw3:\n"
    "s_memtime s[80:81]\n"
    "s_waitcnt lgkmcnt(0)\n"
    "s_sub_u32 s80, s80, s82\n"
    "s_cmp_lt_u32 s80, 16300\n"
    "s_cbranch_scc1 L_lw3\n"
    "buffer_load_dwordx2 v[134:135], v160, s[20:23], 0 offen sc1\n"
    "s_waitcnt vmcnt(6)\n"
    "v_mfma_f32_16x16x32_bf16 v[96:99], a[80:83], v[244:247], v[96:99]\n"
    "s_waitcnt vmcnt(5)\n"
    "v_mfma_f32_16x16x32_bf16 v[96:99], a[96:99], v[248:251], v[96:99]\n"
    "s_waitcnt vmcnt(4)\n"
    "v_mfma_f32_16x16x32_bf16 v[96:99], a[112:115], a[24:27], v[96:99]\n"
    "s_waitcnt vmcnt(3)\n"
    "v_mfma_f32_16x16x32_bf16 v[96:99], a[128:131], a[28:31], v[96:99]\n"
    "v_mfma_f32_16x16x32_bf16 v[100:103], a[36:39], v[146:149], 0\n"
    "v_mfma_f32_16x16x32_bf16 v[100:103], a[52:55], v[178:181], v[100:103]\n"
    "v_mfma_f32_16x16x32_bf16 v[100:103], a[68:71], v[182:185], v[100:103]\n"
    "v_mfma_f32_16x16x32_bf16 v[100:103], a[84:87], v[244:247], v[100:103]\n"
    "v_mfma_f32_16x16x32_bf16 v[100:103], a[100:103], v[248:251], v[100:103]\n"
    "v_mfma_f32_16x16x32_bf16 v[100:103], a[116:119], a[24:27], v[100:103]\n"
    "v_mfma_f32_16x16x32_bf16 v[100:103], a[132:135], a[28:31], v[100:103]\n"
    "v_mfma_f32_16x16x32_bf16 v[104:107], a[40:43], v[146:149], 0\n"
    "v_mfma_f32_16x16x32_bf16 v[104:107], a[56:59], v[178:181], v[104:107]\n"
    "v_mfma_f32_16x16x32_bf16 v[104:107], a[72:75], v[182:185], v[104:107]\n"
    "v_mfma_f32_16x16x32_bf16 v[104:107], a[88:91], v[244:247], v[104:107]\n"
    "v_mfma_f32_16x16x32_bf16 v[104:107], a[104:107], v[248:251], v[104:107]\n"
    "v_mfma_f32_16x16x32_bf16 v[104:107], a[120:123], a[24:27], v[104:107]\n"
    "v_mfma_f32_16x16x32_bf16 v[104:107], a[136:139], a[28:31], v[104:107]\n"
    "v_mfma_f32_16x16x32_bf16 v[108:111], a[44:47], v[146:149], 0\n"
    "v_mfma_f32_16x16x32_bf16 v[108:111], a[60:63], v[178:181], v[108:111]\n"
    "v_mfma_f32_16x16x32_bf16 v[108:111], a[76:79], v[182:185], v[108:111]\n"
    "v_mfma_f32_16x16x32_bf16 v[108:111], a[92:95], v[244:247], v[108:111]\n"
    "v_mfma_f32_16x16x32_bf16 v[108:111], a[108:111], v[248:251], v[108:111]\n"
    "v_mfma_f32_16x16x32_bf16 v[108:111], a[124:127], a[24:27], v[108:111]\n"
    "v_mfma_f32_16x16x32_bf16 v[108:111], a[140:143], a[28:31], v[108:111]\n"
    "s_waitcnt vmcnt(0)\n"
    "v_cmp_ne_u64 vcc, s[56:57], v[134:135]\n"
    "s_cbranch_vccnz L_spin3i\n"
    "L_spin3i_back:\n"
    "v_add_u32 v161, s42, v161\n"
    "v_add_u32 v162, s42, v162\n"
    "buffer_load_dwordx4 v[188:191], v161, s[20:23], 0 offen sc1\n"
    "buffer_load_dwordx4 v[192:195], v161, s[20:23], s58 offen sc1\n"
    "buffer_load_dwordx4 v[196:199], v161, s[20:23], s59 offen sc1\n"
    "buffer_load_dwordx4 v[200:203], v161, s[20:23], s60 offen sc1\n"
    "buffer_load_dwordx4 v[204:207], v161, s[20:23], s61 offen sc1\n"
    "buffer_load_dwordx4 v[208:211], v161, s[20:23], s62 offen sc1\n"
    "buffer_load_dwordx4 v[212:215], v161, s[20:23], s63 offen sc1\n"
    "buffer_load_dwordx4 v[216:219], v162, s[20:23], 0 offen sc1\n"
    "buffer_load_dwordx4 v[220:223], v162, s[20:23], s58 offen sc1\n"
    "buffer_load_dwordx4 v[224:227], v162, s[20:23], s59 offen sc1\n"
    "s_waitcnt vmcnt(9)\n"
    "v_mfma_f32_16x16x32_bf16 v[80:83], a[144:147], v[188:191], v[80:83]\n"
    "v_mfma_f32_16x16x32_bf16 v[84:87], a[148:151], v[188:191], v[84:87]\n"
    "v_mfma_f32_16x16x32_bf16 v[88:91], a[152:155], v[188:191], v[88:91]\n"
    "buffer_load_dwordx4 v[228:231], v162, s[20:23], s60 offen sc1\n"
    "v_mfma_f32_16x16x32_bf16 v[92:95], a[156:159], v[188:191], v[92:95]\n"
    "s_waitcnt vmcnt(9)\n"
    "v_mfma_f32_16x16x32_bf16 v[80:83], a[160:163], v[192:195], v[80:83]\n"
    "v_mfma_f32_16x16x32_bf16 v[84:87], a[164:167], v[192:195], v[84:87]\n"
    "v_mfma_f32_16x16x32_bf16 v[88:91], a[168:171], v[192:195], v[88:91]\n"
    "buffer_load_dwordx4 v[232:235], v162, s[20:23], s61 offen sc1\n"
    "v_mfma_f32_16x16x32_bf16 v[92:95], a[172:175], v[192:195], v[92:95]\n"
    "s_waitcnt vmcnt(9)\n"
    "v_mfma_f32_16x16x32_bf16 v[80:83], a[176:179], v[196:199], v[80:83]\n"
    "v_mfma_f32_16x16x32_bf16 v[84:87], a[180:183], v[196:199], v[84:87]\n"
    "v_mfma_f32_16x16x32_bf16 v[88:91], a[184:187], v[196:199], v[88:91]\n"
    "buffer_load_dwordx4 v[236:239], v162, s[20:23], s62 offen sc1\n"
    "v_mfma_f32_16x16x32_bf16 v[92:95], a[188:191], v[196:199], v[92:95]\n"
    "s_waitcnt vmcnt(9)\n"
    "v_mfma_f32_16x16x32_bf16 v[80:83], a[192:195], v[200:203], v[80:83]\n"
    "v_mfma_f32_16x16x32_bf16 v[84:87], a[196:199], v[200:203], v[84:87]\n"
    "v_mfma_f32_16x16x32_bf16 v[88:91], a[200:203], v[200:203], v[88:91]\n"
    "buffer_load_dwordx4 v[240:243], v162, s[20:23], s63 offen sc1\n"
    "v_mfma_f32_16x16x32_bf16 v[92:95], a[204:207], v[200:203], v[92:95]\n"
    "s_waitcnt vmcnt(9)\n"
    "v_mfma_f32_16x16x32_bf16 v[80:83], a[208:211], v[204:207], v[80:83]\n"
    "v_mfma_f32_16x16x32_bf16 v[84:87], a[212:215], v[204:207], v[84:87]\n"
    "v_mfma_f32_16x16x32_bf16 v[88:91], a[216:219], v[204:207], v[88:91]\n"
    "buffer_load_dwordx4 v[32:35], v253, s[20:23], s40 offen sc1\n"
    "v_mfma_f32_16x16x32_bf16 v[92:95], a[220:223], v[204:207], v[92:95]\n"
    "s_waitcnt vmcnt(9)\n"
    "v_mfma_f32_16x16x32_bf16 v[80:83], a[224:227], v[208:211], v[80:83]\n"
    "v_mfma_f32_16x16x32_bf16 v[84:87], a[228:231], v[208:211], v[84:87]\n"
    "v_mfma_f32_16x16x32_bf16 v[88:91], a[232:235], v[208:211], v[88:91]\n"
    "buffer_load_dwordx4 v[36:39], v253, s[20:23], s41 offen sc1\n"
    "v_mfma_f32_16x16x32_bf16 v[92:95], a[236:239], v[208:211], v[92:95]\n"
    "s_waitcnt vmcnt(9)\n"
    "v_mfma_f32_16x16x32_bf16 v[80:83], a[240:243], v[212:215], v[80:83]\n"
    "v_mfma_f32_16x16x32_bf16 v[84:87], a[244:247], v[212:215], v[84:87]\n"
    "v_mfma_f32_16x16x32_bf16 v[88:91], a[248:251], v[212:215], v[88:91]\n"
    "buffer_load_dword v40, v254, s[20:23], s72 offen sc1\n"
    "v_mfma_f32_16x16x32_bf16 v[92:95], a[252:255], v[212:215], v[92:95]\n"
    "s_waitcnt vmcnt(9)\n"
    "v_mfma_f32_16x16x32_bf16 v[96:99], a[144:147], v[216:219], v[96:99]\n"
    "v_mfma_f32_16x16x32_bf16 v[100:103], a[148:151], v[216:219], v[100:103]\n"
    "v_mfma_f32_16x16x32_bf16 v[104:107], a[152:155], v[216:219], v[104:107]\n"
    "v_mfma_f32_16x16x32_bf16 v[108:111], a[156:159], v[216:219], v[108:111]\n"
    "s_waitcnt vmcnt(8)\n"
    "v_mfma_f32_16x16x32_bf16 v[96:99], a[160:163], v[220:223], v[96:99]\n"
    "ds_write_b128 v175, v[80:83] offset:32768\n"
    "v_mfma_f32_16x16x32_bf16 v[100:103], a[164:167], v[220:223], v[100:103]\n"
    "v_mfma_f32_16x16x32_bf16 v[104:107], a[168:171], v[220:223], v[104:107]\n"
    "ds_write_b128 v175, v[84:87] offset:33792\n"
    "v_mfma_f32_16x16x32_bf16 v[108:111], a[172:175], v[220:223], v[108:111]\n"
    "s_waitcnt vmcnt(7)\n"
    "v_mfma_f32_16x16x32_bf16 v[96:99], a[176:179], v[224:227], v[96:99]\n"
    "ds_write_b128 v175, v[88:91] offset:34816\n"
    "v_mfma_f32_16x16x32_bf16 v[100:103], a[180:183], v[224:227], v[100:103]\n"
    "v_mfma_f32_16x16x32_bf16 v[104:107], a[184:187], v[224:227], v[104:107]\n"
    "ds_write_b128 v175, v[92:95] offset:35840\n"
    "v_mfma_f32_16x16x32_bf16 v[108:111], a[188:191], v[224:227], v[108:111]\n"
    "s_waitcnt vmcnt(6)\n"
    "v_mfma_f32_16x16x32_bf16 v[96:99], a[192:195], v[228:231], v[96:99]\n"
    "v_mfma_f32_16x16x32_bf16 v[100:103], a[196:199], v[228:231], v[100:103]\n"
    "v_mfma_f32_16x16x32_bf16 v[104:107], a[200:203], v[228:231], v[104:107]\n"
    "s_waitcnt lgkmcnt(0)\n"
    "v_mfma_f32_16x16x32_bf16 v[108:111], a[204:207], v[228:231], v[108:111]\n"
    "s_waitcnt vmcnt(5)\n"
    "s_barrier\n"
    "ds_read_b128 v[16:19], v176 offset:32768\n"
    "ds_read_b128 v[20:23], v176 offset:33792\n"
    "v_mfma_f32_16x16x32_bf16 v[96:99], a[208:211], v[232:235], v[96:99]\n"
    "ds_read_b128 v[24:27], v176 offset:34816\n"
    "ds_read_b128 v[28:31], v176 offset:35840\n"
    "v_mfma_f32_16x16x32_bf16 v[100:103], a[212:215], v[232:235], v[100:103]\n"
    "v_mfma_f32_16x16x32_bf16 v[104:107], a[216:219], v[232:235], v[104:107]\n"
    "v_mfma_f32_16x16x32_bf16 v[108:111], a[220:223], v[232:235], v[108:111]\n"
    "s_waitcnt vmcnt(4)\n"
    "v_mfma_f32_16x16x32_bf16 v[96:99], a[224:227], v[236:239], v[96:99]\n"
    "v_mfma_f32_16x16x32_bf16 v[100:103], a[228:231], v[236:239], v[100:103]\n"
    "v_mfma_f32_16x16x32_bf16 v[104:107], a[232:235], v[236:239], v[104:107]\n"
    "v_mfma_f32_16x16x32_bf16 v[108:111], a[236:239], v[236:239], v[108:111]\n"
    "s_waitcnt vmcnt(3)\n"
    "v_mfma_f32_16x16x32_bf16 v[96:99], a[240:243], v[240:243], v[96:99]\n"
    "v_mfma_f32_16x16x32_bf16 v[100:103], a[244:247], v[240:243], v[100:103]\n"
    "v_mfma_f32_16x16x32_bf16 v[104:107], a[248:251], v[240:243], v[104:107]\n"
    "v_mfma_f32_16x16x32_bf16 v[108:111], a[252:255], v[240:243], v[108:111]\n"
    "s_branch L_tail\n"
    "L_spin0:\n"
    "s_mov_b32 s95, 0\n"
    "L_spin0_l:\n"
    "s_sleep 1\n"
    "buffer_load_dwordx2 v[134:135], v157, s[20:23], 0 offen sc1\n"
    "s_waitcnt vmcnt(0)\n"
    "v_cmp_ne_u64 vcc, s[56:57], v[134:135]\n"
    "s_cbranch_vccz L_spin0_back\n"
    "s_add_u32 s95, s95, 1\n"
    "s_cmp_lt_u32 s95, 65536\n"
    "s_cbranch_scc1 L_spin0_l\n"
    "s_branch L_spin0_back\n"
    "L_spin1:\n"
    "s_mov_b32 s95, 0\n"
    "L_spin1_l:\n"
    "s_sleep 1\n"
    "buffer_load_dwordx2 v[134:135], v158, s[20:23], 0 offen sc1\n"
    "s_waitcnt vmcnt(0)\n"
    "v_cmp_ne_u64 vcc, s[56:57], v[134:135]\n"
    "s_cbranch_vccz L_spin1_back\n"
    "s_add_u32 s95, s95, 1\n"
    "s_cmp_lt_u32 s95, 65536\n"
    "s_cbranch_scc1 L_spin1_l\n"
    "s_branch L_spin1_back\n"
    "L_spin2:\n"
    "s_mov_b32 s95, 0\n"
    "L_spin2_l:\n"
    "s_sleep 1\n"
    "buffer_load_dwordx2 v[134:135], v159, s[20:23], 0 offen sc1\n"
    "s_waitcnt vmcnt(0)\n"
    "v_cmp_ne_u64 vcc, s[56:57], v[134:135]\n"
    "s_cbranch_vccz L_spin2_back\n"
    "s_add_u32 s95, s95, 1\n"
    "s_cmp_lt_u32 s95, 65536\n"
    "s_cbranch_scc1 L_spin2_l\n"
    "s_branch L_spin2_back\n"
    "L_spin3:\n"
    "s_mov_b32 s95, 0\n"
    "L_spin3_l:\n"
    "s_sleep 1\n"
    "buffer_load_dwordx2 v[134:135], v160, s[20:23], 0 offen sc1\n"
    "s_waitcnt vmcnt(0)\n"
    "v_cmp_ne_u64 vcc, s[56:57], v[134:135]\n"
    "s_cbranch_vccz L_spin3_back\n"
    "s_add_u32 s95, s95, 1\n"
    "s_cmp_lt_u32 s95, 65536\n"
    "s_cbranch_scc1 L_spin3_l\n"
    "s_branch L_spin3_back\n"
    "L_spin0i:\n"
    "s_mov_b32 s95, 0\n"
    "L_spin0i_l:\n"
    "s_sleep 8\n"
    "buffer_load_dwordx2 v[134:135], v157, s[20:23], 0 offen sc1\n"
    "s_waitcnt vmcnt(0)\n"
    "v_cmp_ne_u64 vcc, s[56:57], v[134:135]\n"
    "s_cbranch_vccz L_spin0i_back\n"
    "s_add_u32 s95, s95, 1\n"
    "s_cmp_lt_u32 s95, 65536\n"
    "s_cbranch_scc1 L_spin0i_l\n"
    "s_branch L_spin0i_back\n"
    "L_spin1i:\n"
    "s_mov_b32 s95, 0\n"
    "L_spin1i_l:\n"
    "s_sleep 8\n"
    "buffer_load_dwordx2 v[134:135], v158, s[20:23], 0 offen sc1\n"
    "s_waitcnt vmcnt(0)\n"
    "v_cmp_ne_u64 vcc, s[56:57], v[134:135]\n"
    "s_cbranch_vccz L_spin1i_back\n"
    "s_add_u32 s95, s95, 1\n"
    "s_cmp_lt_u32 s95, 65536\n"
    "s_cbranch_scc1 L_spin1i_l\n"
    "s_branch L_spin1i_back\n"
    "L_spin2i:\n"
    "s_mov_b32 s95, 0\n"
    "L_spin2i_l:\n"
    "s_sleep 8\n"
    "buffer_load_dwordx2 v[134:135], v159, s[20:23], 0 offen sc1\n"
    "s_waitcnt vmcnt(0)\n"
    "v_cmp_ne_u64 vcc, s[56:57], v[134:135]\n"
    "s_cbranch_vccz L_spin2i_back\n"
    "s_add_u32 s95, s95, 1\n"
    "s_cmp_lt_u32 s95, 65536\n"
    "s_cbranch_scc1 L_spin2i_l\n"
    "s_branch L_spin2i_back\n"
    "L_spin3i:\n"
    "s_mov_b32 s95, 0\n"
    "L_spin3i_l:\n"
    "s_sleep 8\n"
    "buffer_load_dwordx2 v[134:135], v160, s[20:23], 0 offen sc1\n"
    "s_waitcnt vmcnt(0)\n"
    "v_cmp_ne_u64 vcc, s[56:57], v[134:135]\n"
    "s_cbranch_vccz L_spin3i_back\n"
    "s_add_u32 s95, s95, 1\n"
    "s_cmp_lt_u32 s95, 65536\n"
    "s_cbranch_scc1 L_spin3i_l\n"
    "s_branch L_spin3i_back\n"
    "L_pspin:\n"
    "s_mov_b32 s96, 0\n"
    "L_pspin_l:\n"
    "buffer_load_dwordx4 v[32:35], v253, s[20:23], s40 offen sc1\n"
    "buffer_load_dwordx4 v[36:39], v253, s[20:23], s41 offen sc1\n"
    "buffer_load_dword v40, v254, s[20:23], s72 offen sc1\n"
    "s_waitcnt vmcnt(0)\n"
    "v_add3_u32 v112, v132, v32, v33\n"
    "v_add3_u32 v113, v34, v35, v36\n"
    "v_add3_u32 v112, v112, v37, v38\n"
    "v_add3_u32 v112, v112, v113, v39\n"
    "v_cmp_ne_u32 vcc, v112, v40\n"
    "s_cbranch_vccz L_pspin_back\n"
    "s_add_u32 s96, s96, 1\n"
    "s_cmp_lt_u32 s96, 65536\n"
    "s_cbranch_scc1 L_pspin_l\n"
    "s_branch L_pspin_back\n"
    "L_end:\n"
    :
    : "s"(__builtin_amdgcn_kernarg_segment_ptr()), "s"(blockIdx.x), "v"(threadIdx.x), "s"((unsigned)(size_t)lds)
    : "v1", "v2", "v3", "v4", "v5", "v6", "v7", "v8", "v9", "v10", "v11", "v12", "v13", "v14", "v15", "v16", "v17", "v18", "v19", "v20", "v21", "v22", "v23", "v24", "v25", "v26", "v27", "v28", "v29", "v30", "v31", "v32", "v33", "v34", "v35", "v36", "v37", "v38", "v39", "v40", "v41", "v42", "v43", "v44", "v45", "v46", "v47", "v48", "v49", "v50", "v51", "v52", "v53", "v54", "v55", "v56", "v57", "v58", "v59", "v60", "v61", "v62", "v63", "v64", "v65", "v66", "v67", "v68", "v69", "v70", "v71", "v72", "v73", "v74", "v75", "v76", "v77", "v78", "v79", "v80", "v81", "v82", "v83", "v84", "v85", "v86", "v87", "v88", "v89", "v90", "v91", "v92", "v93", "v94", "v95", "v96", "v97", "v98", "v99", "v100", "v101", "v102", "v103", "v104", "v105", "v106", "v107", "v108", "v109", "v110", "v111", "v112", "v113", "v114", "v115", "v116", "v117", "v118", "v119", "v120", "v121", "v122", "v123", "v124", "v125", "v126", "v127", "v128", "v129", "v130", "v131", "v132", "v133", "v134", "v135", "v136", "v137", "v138", "v139", "v140", "v141", "v142", "v143", "v144", "v145", "v146", "v147", "v148", "v149", "v150", "v151", "v152", "v153", "v154", "v155", "v156", "v157", "v158", "v159", "v160", "v161", "v162", "v163", "v164", "v165", "v166", "v167", "v168", "v169", "v170", "v171", "v172", "v173", "v174", "v175", "v176", "v177", "v178", "v179", "v180", "v181", "v182", "v183", "v184", "v185", "v186", "v187", "v188", "v189", "v190", "v191", "v192", "v193", "v194", "v195", "v196", "v197", "v198", "v199", "v200", "v201", "v202", "v203", "v204", "v205", "v206", "v207", "v208", "v209", "v210", "v211", "v212", "v213", "v214", "v215", "v216", "v217", "v218", "v219", "v220", "v221", "v222", "v223", "v224", "v225", "v226", "v227", "v228", "v229", "v230", "v231", "v232", "v233", "v234", "v235", "v236", "v237", "v238", "v239", "v240", "v241", "v242", "v243", "v244", "v245", "v246", "v247", "v248", "v249", "v250", "v251", "v252", "v253", "v254", "v255", "a0", "a1", "a2", "a3", "a4", "a5", "a6", "a7", "a8", "a9", "a10", "a11", "a12", "a13", "a14", "a15", "a16", "a17", "a18", "a19", "a20", "a21", "a22", "a23", "a24", "a25", "a26", "a27", "a28", "a29", "a30", "a31", "a32", "a33", "a34", "a35", "a36", "a37", "a38", "a39", "a40", "a41", "a42", "a43", "a44", "a45", "a46", "a47", "a48", "a49", "a50", "a51", "a52", "a53", "a54", "a55", "a56", "a57", "a58", "a59", "a60", "a61", "a62", "a63", "a64", "a65", "a66", "a67", "a68", "a69", "a70", "a71", "a72", "a73", "a74", "a75", "a76", "a77", "a78", "a79", "a80", "a81", "a82", "a83", "a84", "a85", "a86", "a87", "a88", "a89", "a90", "a91", "a92", "a93", "a94", "a95", "a96", "a97", "a98", "a99", "a100", "a101", "a102", "a103", "a104", "a105", "a106", "a107", "a108", "a109", "a110", "a111", "a112", "a113", "a114", "a115", "a116", "a117", "a118", "a119", "a120", "a121", "a122", "a123", "a124", "a125", "a126", "a127", "a128", "a129", "a130", "a131", "a132", "a133", "a134", "a135", "a136", "a137", "a138", "a139", "a140", "a141", "a142", "a143", "a144", "a145", "a146", "a147", "a148", "a149", "a150", "a151", "a152", "a153", "a154", "a155", "a156", "a157", "a158", "a159", "a160", "a161", "a162", "a163", "a164", "a165", "a166", "a167", "a168", "a169", "a170", "a171", "a172", "a173", "a174", "a175", "a176", "a177", "a178", "a179", "a180", "a181", "a182", "a183", "a184", "a185", "a186", "a187", "a188", "a189", "a190", "a191", "a192", "a193", "a194", "a195", "a196", "a197", "a198", "a199", "a200", "a201", "a202", "a203", "a204", "a205", "a206", "a207", "a208", "a209", "a210", "a211", "a212", "a213", "a214", "a215", "a216", "a217", "a218", "a219", "a220", "a221", "a222", "a223", "a224", "a225", "a226", "a227", "a228", "a229", "a230", "a231", "a232", "a233", "a234", "a235", "a236", "a237", "a238", "a239", "a240", "a241", "a242", "a243", "a244", "a245", "a246", "a247", "a248", "a249", "a250", "a251", "a252", "a253", "a254", "a255", "s4", "s5", "s6", "s7", "s8", "s9", "s10", "s11", "s12", "s13", "s14", "s15", "s16", "s17", "s18", "s19", "s20", "s21", "s22", "s23", "s24", "s25", "s26", "s27", "s28", "s29", "s30", "s31", "s33", "s34", "s35", "s36", "s37", "s38", "s39", "s40", "s41", "s42", "s43", "s44", "s45", "s46", "s47", "s48", "s49", "s50", "s51", "s52", "s53", "s54", "s55", "s56", "s57", "s58", "s59", "s60", "s61", "s62", "s63", "s64", "s65", "s66", "s67", "s68", "s69", "s70", "s71", "s72", "s73", "s74", "s75", "s76", "s77", "s78", "s79", "s80", "s81", "s82", "s83", "s84", "s85", "s86", "s87", "s88", "s89", "s90", "s91", "s92", "s93", "s94", "s95", "s96", "s97", "s98", "s99", "vcc", "scc", "memory");
}

extern "C" void kernel_launch(void* const* d_in, const int* in_sizes, int n_in, void* d_out, int out_size, void* d_ws,
                              size_t ws_size, hipStream_t stream) {
  geglu_ffn_fused<<<256, 256, 0, stream>>>(d_in[0], d_in[1], d_in[2], d_in[3], d_out, d_ws);
}
```
